```python
import torch

def _softmax(x, axis=-1):
    return torch.softmax(x, dim=axis)
def _gelu(x):
    return torch.nn.functional.gelu(x, approximate="tanh")
def _silu(x):
    return torch.nn.functional.silu(x)
def _sigmoid(x):
    return torch.sigmoid(x)
def _layernorm(x, axis=-1, eps=1e-5):
    m = x.mean(dim=axis, keepdim=True)
    v = x.var(dim=axis, keepdim=True, unbiased=False)
    return (x - m) / torch.sqrt(v + eps)
def _scatter_add(tgt, idx, src, axis=0):
    return tgt.clone().index_add_(axis, idx, src)
def _segment_sum(src, seg, n):
    out = torch.zeros((n,) + src.shape[1:], dtype=src.dtype)
    return out.index_add_(0, seg, src)
def _pad(x, pw):
    flat = tuple(v for pair in reversed(pw) for v in pair)
    return torch.nn.functional.pad(x, flat)

kernel_name = 'standalone__swiglu_ffn__sampling'

def setup_inputs():

    _w = (lambda a: (lambda t: t.to(torch.bfloat16) if t.is_floating_point() else t)(torch.as_tensor(a)))
    B, S, D, F = 16, 16, 1024, 3584
    return {
        "x": _w((lambda *s: torch.randn(*s, dtype=torch.bfloat16) * 0.05)(B, S, D)),
        "w_gate": _w((lambda *s: torch.randn(*s, dtype=torch.bfloat16) * 0.05)(D, F)),
        "w_up": _w((lambda *s: torch.randn(*s, dtype=torch.bfloat16) * 0.05)(D, F)),
        "w_down": _w((lambda *s: torch.randn(*s, dtype=torch.bfloat16) * 0.05)(F, D)),
    }

def reference(x, w_gate, w_up, w_down):
    g = _silu(x @ w_gate)
    u = x @ w_up
    return (g * u) @ w_down
```

```cpp
#include <hip/hip_runtime.h>
#include <hip/hip_bf16.h>
#include <stdint.h>

typedef unsigned v4u __attribute__((ext_vector_type(4)));
typedef unsigned v2u __attribute__((ext_vector_type(2)));
typedef float f32x4 __attribute__((ext_vector_type(4)));
typedef short bf16x8 __attribute__((ext_vector_type(8)));
#define GAS __attribute__((address_space(1)))
#define RLX_AGENT __ATOMIC_RELAXED, __HIP_MEMORY_SCOPE_AGENT

constexpr int M_ = 256, D_ = 1024, F_ = 3584;
constexpr int KS2 = F_ / 32;

constexpr size_t CNT_OFF = 0;
constexpr size_t H_OFF = 16384;
constexpr size_t H_BYTES = (size_t)M_ * F_ * 2;
constexpr size_t SLAB_OFF = H_OFF + H_BYTES;
constexpr size_t SLAB_BYTES = (size_t)64 * 4 * 16384;
constexpr size_t WS_BYTES = SLAB_OFF + SLAB_BYTES;

__device__ __forceinline__ f32x4 mfma16(v4u a, v4u b, f32x4 c) {
  return __builtin_amdgcn_mfma_f32_16x16x32_bf16(__builtin_bit_cast(bf16x8, a), __builtin_bit_cast(bf16x8, b), c, 0, 0, 0);
}
__device__ __forceinline__ float bf16_round(float f) {
  __hip_bfloat16 b = __float2bfloat16(f);
  return __bfloat162float(b);
}
__device__ __forceinline__ unsigned f2bf_bits(float f) {
  __hip_bfloat16 b = __float2bfloat16(f);
  return (unsigned)__builtin_bit_cast(unsigned short, b);
}
__device__ __forceinline__ void transpose8(const v4u (&w)[8], v4u (&f)[8]) {
#pragma unroll
  for (int d = 0; d < 4; ++d) {
#pragma unroll
    for (int t = 0; t < 4; ++t) {
      const unsigned lo = w[2 * t][d], hi = w[2 * t + 1][d];
      f[2 * d][t] = __builtin_amdgcn_perm(hi, lo, 0x05040100u);
      f[2 * d + 1][t] = __builtin_amdgcn_perm(hi, lo, 0x07060302u);
    }
  }
}

constexpr int WIN1 = 5;
__global__ void __launch_bounds__(256, 1)
k_gateup(const unsigned short* __restrict__ x, const unsigned short* __restrict__ wg,
         const unsigned short* __restrict__ wu, unsigned char* __restrict__ ws) {
  __shared__ f32x4 red[4 * 3 * 8 * 64];
  const int tid = threadIdx.x, lane = tid & 63, w = __builtin_amdgcn_readfirstlane(tid >> 6);
  const int i = lane & 15, g = lane >> 4;
  const int bid = blockIdx.x, xcd = bid & 7, r = bid >> 3;
  const int mt = r & 3, nt = xcd * 7 + (r >> 2);
  if (bid == 0 && tid < 64) ((unsigned*)(ws + CNT_OFF))[tid * 32] = 0u;

  const unsigned short* wp = (i < 8 ? wg : wu) + (size_t)(256 * w + 8 * g) * F_ + nt * 64 + (i & 7) * 8;
  const unsigned short* xp = x + (size_t)(mt * 64 + i) * D_ + 256 * w + 8 * g;

  v4u wb[WIN1][8];
  v4u xb[WIN1][4];
#define ISSUE1(s)                                                                          \
  {                                                                                        \
    _Pragma("unroll") for (int j = 0; j < 8; ++j)                                          \
        wb[(s) % WIN1][j] = *(const v4u*)(wp + (size_t)(32 * (s) + j) * F_);               \
    _Pragma("unroll") for (int t = 0; t < 4; ++t)                                          \
        xb[(s) % WIN1][t] = *(const v4u*)(xp + (size_t)(16 * t) * D_ + 32 * (s));          \
  }
#pragma unroll
  for (int s = 0; s < WIN1; ++s) ISSUE1(s)
  __builtin_amdgcn_sched_barrier(0);

  f32x4 acc[4][8];
#pragma unroll
  for (int t = 0; t < 4; ++t)
#pragma unroll
    for (int c = 0; c < 8; ++c) acc[t][c] = f32x4{0.f, 0.f, 0.f, 0.f};

#pragma unroll
  for (int s = 0; s < 8; ++s) {
    v4u f[8];
    transpose8(wb[s % WIN1], f);
    v4u xa[4];
#pragma unroll
    for (int t = 0; t < 4; ++t) xa[t] = xb[s % WIN1][t];
    __builtin_amdgcn_sched_barrier(0);
    if (s + WIN1 < 8) ISSUE1(s + WIN1)
    __builtin_amdgcn_sched_barrier(0);
#pragma unroll
    for (int c = 0; c < 8; ++c)
#pragma unroll
      for (int t = 0; t < 4; ++t) acc[t][c] = mfma16(xa[t], f[c], acc[t][c]);
    __builtin_amdgcn_sched_barrier(0);
  }

#pragma unroll
  for (int t = 0; t < 4; ++t) {
    if (t != w) {
      const int slot = t < w ? t : t - 1;
#pragma unroll
      for (int c = 0; c < 8; ++c) red[((w * 3 + slot) * 8 + c) * 64 + lane] = acc[t][c];
    }
  }
  __syncthreads();
  f32x4 fin[8];
#pragma unroll
  for (int c = 0; c < 8; ++c) {
    f32x4 own = w == 0 ? acc[0][c] : w == 1 ? acc[1][c] : w == 2 ? acc[2][c] : acc[3][c];
    f32x4 sum = f32x4{0.f, 0.f, 0.f, 0.f};
#pragma unroll
    for (int ww = 0; ww < 4; ++ww) {
      if (ww == w) sum += own;
      else {
        const int slot = w < ww ? w : w - 1;
        sum += red[((ww * 3 + slot) * 8 + c) * 64 + lane];
      }
    }
    fin[c] = sum;
  }
  const bool lo = i < 8;
  unsigned hv[2][4];
#pragma unroll
  for (int rr = 0; rr < 2; ++rr) {
    float h[8];
#pragma unroll
    for (int c = 0; c < 8; ++c) {
      const float send = lo ? fin[c][2 + rr] : fin[c][rr];
      const float recv = __builtin_bit_cast(float, __builtin_amdgcn_update_dpp(0, __builtin_bit_cast(int, send), 0x128, 0xf, 0xf, false));
      const float gpre = lo ? fin[c][rr] : recv;
      const float upre = lo ? recv : fin[c][2 + rr];
      const float gb = bf16_round(gpre);
      const float sg = bf16_round(gb / (1.0f + __expf(-gb)));
      const float ub = bf16_round(upre);
      h[c] = sg * ub;
    }
#pragma unroll
    for (int d = 0; d < 4; ++d) hv[rr][d] = f2bf_bits(h[2 * d]) | (f2bf_bits(h[2 * d + 1]) << 16);
  }
  {
    const int mtile16 = mt * 4 + w, ks = 2 * nt + ((i & 7) >> 2);
    unsigned char* hb = ws + H_OFF + ((size_t)mtile16 * KS2 + ks) * 1024;
#pragma unroll
    for (int rr = 0; rr < 2; ++rr) {
      const int row = 4 * g + (lo ? 0 : 2) + rr;
      *(v4u*)(hb + ((i & 3) * 16 + row) * 16) = v4u{hv[rr][0], hv[rr][1], hv[rr][2], hv[rr][3]};
    }
  }
}

__global__ void __launch_bounds__(256, 1)
k_down(const unsigned short* __restrict__ wd, unsigned char* __restrict__ ws, unsigned short* __restrict__ out) {
  __shared__ f32x4 red[4 * 12 * 64];
  __shared__ unsigned flag_last;
  const int tid = threadIdx.x, lane = tid & 63, w = __builtin_amdgcn_readfirstlane(tid >> 6);
  const int i = lane & 15, g = lane >> 4;
  const int bid = blockIdx.x, xcd = bid & 7, r = bid >> 3;
  const int mt = r & 7, nt = xcd, s = r >> 3;
  const int ks0 = 28 * s + 7 * w;

  const unsigned short* wp = wd + (size_t)(32 * ks0 + 8 * g) * D_ + nt * 128 + i * 8;
  const unsigned char* hp = ws + H_OFF + ((size_t)(2 * mt) * KS2 + ks0) * 1024 + lane * 16;

  v4u wb[7][8];
  v4u hb[7][2];
#pragma unroll
  for (int q = 0; q < 7; ++q) {
#pragma unroll
    for (int j = 0; j < 8; ++j) wb[q][j] = *(const v4u*)(wp + (size_t)(32 * q + j) * D_);
#pragma unroll
    for (int t = 0; t < 2; ++t) hb[q][t] = *(const v4u*)(hp + (size_t)t * KS2 * 1024 + q * 1024);
  }
  __builtin_amdgcn_sched_barrier(0);
  f32x4 acc[2][8];
#pragma unroll
  for (int t = 0; t < 2; ++t)
#pragma unroll
    for (int c = 0; c < 8; ++c) acc[t][c] = f32x4{0.f, 0.f, 0.f, 0.f};
#pragma unroll
  for (int q = 0; q < 7; ++q) {
    v4u f[8];
    transpose8(wb[q], f);
#pragma unroll
    for (int c = 0; c < 8; ++c)
#pragma unroll
      for (int t = 0; t < 2; ++t) acc[t][c] = mfma16(hb[q][t], f[c], acc[t][c]);
    __builtin_amdgcn_sched_barrier(0);
  }
#pragma unroll
  for (int o = 0; o < 4; ++o) {
    if (o != w) {
      const int slot = o < w ? o : o - 1;
#pragma unroll
      for (int cc = 0; cc < 4; ++cc) red[((w * 3 + slot) * 4 + cc) * 64 + lane] = acc[o >> 1][4 * (o & 1) + cc];
    }
  }
  __syncthreads();
  f32x4 fin[4];
#pragma unroll
  for (int cc = 0; cc < 4; ++cc) {
    f32x4 own = w == 0 ? acc[0][cc] : w == 1 ? acc[0][4 + cc] : w == 2 ? acc[1][cc] : acc[1][4 + cc];
    f32x4 sum = f32x4{0.f, 0.f, 0.f, 0.f};
#pragma unroll
    for (int ww = 0; ww < 4; ++ww) {
      if (ww == w) sum += own;
      else {
        const int slot = w < ww ? w : w - 1;
        sum += red[((ww * 3 + slot) * 4 + cc) * 64 + lane];
      }
    }
    fin[cc] = sum;
  }
  const __amdgpu_buffer_rsrc_t rs = __builtin_amdgcn_make_buffer_rsrc((void*)ws, 0, (int)WS_BYTES, 0x00020000);
  const int tile = mt * 8 + nt;
  const int so = (int)SLAB_OFF + tile * 65536 + (w * 4 * 64 + lane) * 16;
#pragma unroll
  for (int cc = 0; cc < 4; ++cc)
    __builtin_amdgcn_raw_buffer_store_b128(__builtin_bit_cast(v4u, fin[cc]), rs, so + s * 16384 + cc * 1024, 0, 16);
  asm volatile("s_waitcnt vmcnt(0)" ::: "memory");
  __syncthreads();
  if (tid == 0) {
    const unsigned old = __hip_atomic_fetch_add((GAS unsigned*)(ws + CNT_OFF) + tile * 32, 1u, RLX_AGENT);
    __builtin_amdgcn_fence(__ATOMIC_ACQUIRE, "wavefront");
    flag_last = (old == 3u) ? 1u : 0u;
  }
  __syncthreads();
  if (flag_last) {
    v4u sl[4][4];
#pragma unroll
    for (int ss = 0; ss < 4; ++ss)
#pragma unroll
      for (int cc = 0; cc < 4; ++cc)
        sl[ss][cc] = __builtin_amdgcn_raw_buffer_load_b128(rs, so + ss * 16384 + cc * 1024, 0, 16);
    f32x4 o[4];
#pragma unroll
    for (int cc = 0; cc < 4; ++cc) {
      o[cc] = __builtin_bit_cast(f32x4, sl[0][cc]);
#pragma unroll
      for (int ss = 1; ss < 4; ++ss) o[cc] += __builtin_bit_cast(f32x4, sl[ss][cc]);
    }
    const int t = w >> 1, c0 = 4 * (w & 1);
#pragma unroll
    for (int rr = 0; rr < 4; ++rr) {
      const int row = mt * 32 + 16 * t + 4 * g + rr;
      v2u v;
      v.x = f2bf_bits(o[0][rr]) | (f2bf_bits(o[1][rr]) << 16);
      v.y = f2bf_bits(o[2][rr]) | (f2bf_bits(o[3][rr]) << 16);
      *(v2u*)(out + (size_t)row * D_ + nt * 128 + 8 * i + c0) = v;
    }
  }
}

extern "C" void kernel_launch(void* const* d_in, const int* in_sizes, int n_in, void* d_out, int out_size,
                              void* d_ws, size_t ws_size, hipStream_t stream) {
  const unsigned short* x = (const unsigned short*)d_in[0];
  const unsigned short* wg = (const unsigned short*)d_in[1];
  const unsigned short* wu = (const unsigned short*)d_in[2];
  const unsigned short* wd = (const unsigned short*)d_in[3];
  k_gateup<<<224, 256, 0, stream>>>(x, wg, wu, (unsigned char*)d_ws);
  k_down<<<256, 256, 0, stream>>>(wd, (unsigned char*)d_ws, (unsigned short*)d_out);
}
```

```cpp
#include <hip/hip_runtime.h>
#include <hip/hip_bf16.h>
#include <stdint.h>

typedef unsigned v4u __attribute__((ext_vector_type(4)));
typedef unsigned v2u __attribute__((ext_vector_type(2)));
typedef float f32x4 __attribute__((ext_vector_type(4)));
typedef short bf16x8 __attribute__((ext_vector_type(8)));
#define GAS __attribute__((address_space(1)))
#define RLX_AGENT __ATOMIC_RELAXED, __HIP_MEMORY_SCOPE_AGENT
#define SB() __builtin_amdgcn_sched_barrier(0)

#define STAMP(n)

constexpr int M_ = 256, D_ = 1024, F_ = 3584;
constexpr int KS2 = F_ / 32;

constexpr size_t CNT_OFF = 0;
constexpr size_t H_OFF = 16384;
constexpr size_t H_BYTES = (size_t)M_ * F_ * 2;
constexpr size_t SLAB_OFF = H_OFF + H_BYTES;
constexpr size_t SLAB_BYTES = (size_t)64 * 4 * 16384;
constexpr size_t WS_BYTES = SLAB_OFF + SLAB_BYTES;

__device__ __forceinline__ f32x4 mfma16(v4u a, v4u b, f32x4 c) {
  return __builtin_amdgcn_mfma_f32_16x16x32_bf16(__builtin_bit_cast(bf16x8, a), __builtin_bit_cast(bf16x8, b), c, 0, 0, 0);
}
__device__ __forceinline__ float bf16_round(float f) {
  __hip_bfloat16 b = __float2bfloat16(f);
  return __bfloat162float(b);
}
__device__ __forceinline__ unsigned f2bf_bits(float f) {
  __hip_bfloat16 b = __float2bfloat16(f);
  return (unsigned)__builtin_bit_cast(unsigned short, b);
}
__device__ __forceinline__ v4u colfrag(const v4u (&w)[8], int c) {
  v4u f;
  const unsigned sel = (c & 1) ? 0x07060302u : 0x05040100u;
#pragma unroll
  for (int t = 0; t < 4; ++t) f[t] = __builtin_amdgcn_perm(w[2 * t + 1][c >> 1], w[2 * t][c >> 1], sel);
  return f;
}

__global__ void __launch_bounds__(256, 1)
k_gateup(const unsigned short* __restrict__ x, const unsigned short* __restrict__ wg,
         const unsigned short* __restrict__ wu, unsigned char* __restrict__ ws) {
  __shared__ f32x4 red[4 * 3 * 8 * 64];
  const int tid = threadIdx.x, lane = tid & 63, w = __builtin_amdgcn_readfirstlane(tid >> 6);
  const int i = lane & 15, g = lane >> 4;
  const int bid = blockIdx.x, xcd = bid & 7, r = bid >> 3;
  const int mt = r & 3, nt = xcd * 7 + (r >> 2);
  if (bid == 0 && tid < 64) ((unsigned*)(ws + CNT_OFF))[tid * 32] = 0u;
  STAMP(0)

  const unsigned short* wp = (i < 8 ? wg : wu) + (size_t)(256 * w + 16 * g) * F_ + nt * 64 + (i & 7) * 8;
  const unsigned short* xp[4];
#pragma unroll
  for (int t = 0; t < 4; ++t) xp[t] = x + (size_t)(mt * 64 + 16 * ((t + w) & 3) + i) * D_ + 256 * w + 16 * g;
  const int rot = mt;

  v4u wb[2][2][8];
  v4u xb[2][2][4];
#define LDW(p, h, j) wb[(p) & 1][h][j] = *(const v4u*)(wp + (size_t)(64 * (((p) + rot) & 3) + 8 * (h) + (j)) * F_)
#define LDX(p, h, t) xb[(p) & 1][h][t] = *(const v4u*)(xp[t] + 64 * (((p) + rot) & 3) + 8 * (h))
#pragma unroll
  for (int t = 0; t < 4; ++t) { LDX(0, 0, t); LDX(0, 1, t); }
#pragma unroll
  for (int h = 0; h < 2; ++h)
#pragma unroll
    for (int j = 0; j < 8; ++j) LDW(0, h, j);
  SB();
  STAMP(1)

  f32x4 acc[4][8];
#pragma unroll
  for (int t = 0; t < 4; ++t)
#pragma unroll
    for (int c = 0; c < 8; ++c) acc[t][c] = f32x4{0.f, 0.f, 0.f, 0.f};

#pragma unroll
  for (int p = 0; p < 4; ++p) {
#pragma unroll
    for (int h = 0; h < 2; ++h) {
#pragma unroll
      for (int c = 0; c < 8; ++c) {
        if (p + 1 < 4) {
          if (h == 0 && c < 4) { LDX(p + 1, 0, c); LDX(p + 1, 1, c); }
          if (h == 0 && c >= 4) { LDW(p + 1, 0, 2 * (c - 4)); LDW(p + 1, 0, 2 * (c - 4) + 1); }
          if (h == 1) LDW(p + 1, 1, c);
        }
        const v4u f = colfrag(wb[p & 1][h], c);
#pragma unroll
        for (int t = 0; t < 4; ++t) acc[t][c] = mfma16(xb[p & 1][h][t], f, acc[t][c]);
        SB();
      }
      STAMP(2 + 2 * p + h)
    }
  }

#pragma unroll
  for (int t = 1; t < 4; ++t) {
    const int dw = (w + t) & 3;
#pragma unroll
    for (int c = 0; c < 8; ++c) red[((dw * 3 + (t - 1)) * 8 + c) * 64 + lane] = acc[t][c];
  }
  __syncthreads();
  f32x4 fin[8];
#pragma unroll
  for (int c = 0; c < 8; ++c) {
    f32x4 sum = acc[0][c];
#pragma unroll
    for (int sl = 0; sl < 3; ++sl) sum += red[((w * 3 + sl) * 8 + c) * 64 + lane];
    fin[c] = sum;
  }
  STAMP(10)
  const bool lo = i < 8;
  unsigned hv[2][4];
#pragma unroll
  for (int rr = 0; rr < 2; ++rr) {
    float h[8];
#pragma unroll
    for (int c = 0; c < 8; ++c) {
      const float send = lo ? fin[c][2 + rr] : fin[c][rr];
      const float recv = __builtin_bit_cast(float, __builtin_amdgcn_update_dpp(0, __builtin_bit_cast(int, send), 0x128, 0xf, 0xf, false));
      const float gpre = lo ? fin[c][rr] : recv;
      const float upre = lo ? recv : fin[c][2 + rr];
      const float gb = bf16_round(gpre);
      const float sg = bf16_round(gb * __builtin_amdgcn_rcpf(1.0f + __expf(-gb)));
      const float ub = bf16_round(upre);
      h[c] = sg * ub;
    }
#pragma unroll
    for (int d = 0; d < 4; ++d) hv[rr][d] = f2bf_bits(h[2 * d]) | (f2bf_bits(h[2 * d + 1]) << 16);
  }
  {
    const int mtile16 = mt * 4 + w, ks = 2 * nt + ((i & 7) >> 2);
    unsigned char* hb = ws + H_OFF + ((size_t)mtile16 * KS2 + ks) * 1024;
#pragma unroll
    for (int rr = 0; rr < 2; ++rr) {
      const int row = 4 * g + (lo ? 0 : 2) + rr;
      *(v4u*)(hb + ((i & 3) * 16 + row) * 16) = v4u{hv[rr][0], hv[rr][1], hv[rr][2], hv[rr][3]};
    }
  }
  STAMP(11)
}

__global__ void __launch_bounds__(256, 1)
k_down(const unsigned short* __restrict__ wd, unsigned char* __restrict__ ws, unsigned short* __restrict__ out) {
  __shared__ f32x4 red[4 * 12 * 64 + 1];
  const int tid = threadIdx.x, lane = tid & 63, w = __builtin_amdgcn_readfirstlane(tid >> 6);
  const int i = lane & 15, g = lane >> 4;
  const int bid = blockIdx.x, xcd = bid & 7, r = bid >> 3;
  const int mt = r & 7, nt = xcd, s = r >> 3;
  const int ks0 = 28 * s + 7 * w;
  const int rot = mt == 7 ? 0 : mt;

  const unsigned short* wp = wd + (size_t)(32 * ks0 + 8 * g) * D_ + nt * 128 + i * 8;
  const unsigned char* hp = ws + H_OFF + ((size_t)(2 * mt) * KS2 + ks0) * 1024 + lane * 16;

  v4u wb[3][8];
  v4u hb[3][2];
#define PQ(q) (((q) + rot) >= 7 ? (q) + rot - 7 : (q) + rot)
#define LDW2(q, j) wb[(q) % 3][j] = *(const v4u*)(wp + (size_t)(32 * PQ(q) + (j)) * D_)
#define LDH2(q, t) hb[(q) % 3][t] = *(const v4u*)(hp + (size_t)(t) * KS2 * 1024 + PQ(q) * 1024)
#pragma unroll
  for (int q = 0; q < 2; ++q) {
#pragma unroll
    for (int j = 0; j < 8; ++j) LDW2(q, j);
#pragma unroll
    for (int t = 0; t < 2; ++t) LDH2(q, t);
    SB();
  }
  f32x4 acc[2][8];
#pragma unroll
  for (int t = 0; t < 2; ++t)
#pragma unroll
    for (int c = 0; c < 8; ++c) acc[t][c] = f32x4{0.f, 0.f, 0.f, 0.f};
#pragma unroll
  for (int q = 0; q < 7; ++q) {
#pragma unroll
    for (int c = 0; c < 8; ++c) {
      if (q + 2 < 7) {
        LDW2(q + 2, c);
        if ((c & 3) == 1) LDH2(q + 2, c >> 2);
      }
      const v4u f = colfrag(wb[q % 3], c);
#pragma unroll
      for (int t = 0; t < 2; ++t) acc[t][c] = mfma16(hb[q % 3][t], f, acc[t][c]);
      SB();
    }
  }
#pragma unroll
  for (int o = 0; o < 4; ++o) {
    if (o != w) {
      const int slot = o < w ? o : o - 1;
#pragma unroll
      for (int cc = 0; cc < 4; ++cc) red[((w * 3 + slot) * 4 + cc) * 64 + lane] = acc[o >> 1][4 * (o & 1) + cc];
    }
  }
  __syncthreads();
  f32x4 fin[4];
#pragma unroll
  for (int cc = 0; cc < 4; ++cc) {
    f32x4 own = w == 0 ? acc[0][cc] : w == 1 ? acc[0][4 + cc] : w == 2 ? acc[1][cc] : acc[1][4 + cc];
    f32x4 sum = f32x4{0.f, 0.f, 0.f, 0.f};
#pragma unroll
    for (int ww = 0; ww < 4; ++ww) {
      if (ww == w) sum += own;
      else {
        const int slot = w < ww ? w : w - 1;
        sum += red[((ww * 3 + slot) * 4 + cc) * 64 + lane];
      }
    }
    fin[cc] = sum;
  }
  const __amdgpu_buffer_rsrc_t rs = __builtin_amdgcn_make_buffer_rsrc((void*)ws, 0, (int)WS_BYTES, 0x00020000);
  const int tile = mt * 8 + nt;
  const int so = (int)SLAB_OFF + tile * 65536 + (w * 4 * 64 + lane) * 16;
#pragma unroll
  for (int cc = 0; cc < 4; ++cc)
    __builtin_amdgcn_raw_buffer_store_b128(__builtin_bit_cast(v4u, fin[cc]), rs, so + s * 16384 + cc * 1024, 0, 16);
  asm volatile("s_waitcnt vmcnt(0)" ::: "memory");
  __syncthreads();
  volatile unsigned* flag_last = (volatile unsigned*)&red[4 * 12 * 64];
  if (tid == 0) {
    const unsigned old = __hip_atomic_fetch_add((GAS unsigned*)(ws + CNT_OFF) + tile * 32, 1u, RLX_AGENT);
    __builtin_amdgcn_fence(__ATOMIC_ACQUIRE, "wavefront");
    *flag_last = (old == 3u) ? 1u : 0u;
  }
  __syncthreads();
  if (*flag_last) {
    v4u sl[4][4];
#pragma unroll
    for (int ss = 0; ss < 4; ++ss)
#pragma unroll
      for (int cc = 0; cc < 4; ++cc)
        sl[ss][cc] = __builtin_amdgcn_raw_buffer_load_b128(rs, so + ss * 16384 + cc * 1024, 0, 16);
    f32x4 o[4];
#pragma unroll
    for (int cc = 0; cc < 4; ++cc) {
      o[cc] = __builtin_bit_cast(f32x4, sl[0][cc]);
#pragma unroll
      for (int ss = 1; ss < 4; ++ss) o[cc] += __builtin_bit_cast(f32x4, sl[ss][cc]);
    }
    const int t = w >> 1, c0 = 4 * (w & 1);
#pragma unroll
    for (int rr = 0; rr < 4; ++rr) {
      const int row = mt * 32 + 16 * t + 4 * g + rr;
      v2u v;
      v.x = f2bf_bits(o[0][rr]) | (f2bf_bits(o[1][rr]) << 16);
      v.y = f2bf_bits(o[2][rr]) | (f2bf_bits(o[3][rr]) << 16);
      *(v2u*)(out + (size_t)row * D_ + nt * 128 + 8 * i + c0) = v;
    }
  }
}

extern "C" void kernel_launch(void* const* d_in, const int* in_sizes, int n_in, void* d_out, int out_size,
                              void* d_ws, size_t ws_size, hipStream_t stream) {
  const unsigned short* x = (const unsigned short*)d_in[0];
  const unsigned short* wg = (const unsigned short*)d_in[1];
  const unsigned short* wu = (const unsigned short*)d_in[2];
  const unsigned short* wd = (const unsigned short*)d_in[3];
  k_gateup<<<224, 256, 0, stream>>>(x, wg, wu, (unsigned char*)d_ws);
  k_down<<<256, 256, 0, stream>>>(wd, (unsigned char*)d_ws, (unsigned short*)d_out);
}
```

```cpp
#include <hip/hip_runtime.h>
#include <hip/hip_bf16.h>
#include <stdint.h>

typedef unsigned v4u __attribute__((ext_vector_type(4)));
typedef unsigned v2u __attribute__((ext_vector_type(2)));
typedef float f32x4 __attribute__((ext_vector_type(4)));
typedef short bf16x8 __attribute__((ext_vector_type(8)));
typedef unsigned long long u64;
#define GAS __attribute__((address_space(1)))
#define LAS __attribute__((address_space(3)))
#define RLX_AGENT __ATOMIC_RELAXED, __HIP_MEMORY_SCOPE_AGENT
#define SB() __builtin_amdgcn_sched_barrier(0)
#define BAR() asm volatile("s_waitcnt lgkmcnt(0)\n\ts_barrier" ::: "memory")
#define VMWAIT(n) asm volatile("s_waitcnt vmcnt(" #n ")" ::: "memory")

#define STAMP(n)

constexpr int M_ = 256, D_ = 1024, F_ = 3584;
constexpr int KS2 = F_ / 32;

constexpr size_t EP_OFF = 0;
constexpr size_t F1_OFF = 256;
constexpr size_t F2_OFF = 4096;
constexpr size_t H_OFF = 16384;
constexpr size_t H_BYTES = (size_t)M_ * F_ * 2;
constexpr size_t SLAB_OFF = H_OFF + H_BYTES;
constexpr size_t SLAB_BYTES = (size_t)64 * 16 * 4096;
constexpr size_t WS_BYTES = SLAB_OFF + SLAB_BYTES;
constexpr unsigned SPIN_CAP = 1u << 20;

__device__ __forceinline__ f32x4 mfma16(v4u a, v4u b, f32x4 c) {
  return __builtin_amdgcn_mfma_f32_16x16x32_bf16(__builtin_bit_cast(bf16x8, a), __builtin_bit_cast(bf16x8, b), c, 0, 0, 0);
}
__device__ __forceinline__ float bf16_round(float f) {
  __hip_bfloat16 b = __float2bfloat16(f);
  return __bfloat162float(b);
}
__device__ __forceinline__ unsigned f2bf_bits(float f) {
  __hip_bfloat16 b = __float2bfloat16(f);
  return (unsigned)__builtin_bit_cast(unsigned short, b);
}
__device__ __forceinline__ v4u colfrag(const v4u (&w)[8], int c) {
  v4u f;
  const unsigned sel = (c & 1) ? 0x07060302u : 0x05040100u;
#pragma unroll
  for (int t = 0; t < 4; ++t) f[t] = __builtin_amdgcn_perm(w[2 * t + 1][c >> 1], w[2 * t][c >> 1], sel);
  return f;
}
__device__ __forceinline__ void glds16(unsigned voff, const void* sbase, unsigned ldst) {
  unsigned keep;
  asm volatile("s_mov_b32 %0, m0\n\ts_mov_b32 m0, %3\n\ts_nop 0\n\tglobal_load_lds_dwordx4 %1, %2\n\ts_mov_b32 m0, %0"
               : "=&s"(keep) : "v"(voff), "s"(sbase), "s"(ldst) : "memory");
}

__global__ void __launch_bounds__(256, 1)
k_fused(const unsigned short* __restrict__ x, const unsigned short* __restrict__ wg,
        const unsigned short* __restrict__ wu, const unsigned short* __restrict__ wd,
        unsigned char* __restrict__ ws, unsigned short* __restrict__ out) {
  __shared__ __attribute__((aligned(16))) unsigned char smem[131072];
  const int tid = threadIdx.x, lane = tid & 63, w = __builtin_amdgcn_readfirstlane(tid >> 6);
  const int i = lane & 15, g = lane >> 4;
  const int bid = blockIdx.x, xcd = bid & 7, r = bid >> 3;
  STAMP(0)
  const u64 ep = __hip_atomic_load((GAS u64*)(ws + EP_OFF), RLX_AGENT);
  const __amdgpu_buffer_rsrc_t rs = __builtin_amdgcn_make_buffer_rsrc((void*)ws, 0, (int)WS_BYTES, 0x00020000);
  const __amdgpu_buffer_rsrc_t rd = __builtin_amdgcn_make_buffer_rsrc((void*)wd, 0, F_ * D_ * 2, 0x00020000);

  const int mt2 = r & 7, nt2 = xcd, s2 = r >> 3;
  const int ks0 = 28 * s2 + 7 * w;
  const int rot2 = mt2 == 7 ? 0 : mt2;
  const int wdv = ((8 * g) * D_ + nt2 * 128 + i * 8) * 2;
  v4u wdb[7][8];
#define PQ(q) (((q) + rot2) >= 7 ? (q) + rot2 - 7 : (q) + rot2)
#define LDW2(q, j) wdb[q][j] = __builtin_amdgcn_raw_buffer_load_b128(rd, wdv, (32 * (ks0 + PQ(q)) + (j)) * (D_ * 2), 0)

  if (bid < 224) {
    const int mt = r & 3, nt = xcd * 7 + (r >> 2);
    const unsigned lds0 = (unsigned)(size_t)(LAS unsigned char*)smem;
    const unsigned xreg = lds0 + w * 32768;
    const int r8 = lane >> 3;
    unsigned xv[2];
#pragma unroll
    for (int par = 0; par < 2; ++par) xv[par] = ((8 * par + r8) * D_ + (((lane & 7) ^ ((par << 2) | (r8 >> 1))) * 8)) * 2;
    const unsigned short* xw = x + (size_t)(mt * 64) * D_ + 256 * w;
#define XP(j) ((mt + (j)) & 3)
#define LDXD(j, R) glds16(xv[(R) & 1], xw + (size_t)(16 * ((R) >> 1)) * D_ + 64 * XP(j), xreg + XP(j) * 8192 + (R) * 1024)
    int pc[8];
#pragma unroll
    for (int s = 0; s < 8; ++s) {
      int p;
      if (s == 0) p = mt;
      else if (s >= 5) p = 4 + ((mt + s - 4) & 3);
      else p = (s == w + 1) ? 4 + mt : (s <= w ? ((mt + s) & 3) : ((mt + s - 1) & 3));
      pc[s] = p;
    }
    const unsigned short* wp = (i < 8 ? wg : wu) + (size_t)(256 * w + 8 * g) * F_ + nt * 64 + (i & 7) * 8;
    v4u wb[3][8];
#define LDW(s, j) wb[(s) % 3][j] = *(const v4u*)(wp + (size_t)(64 * (pc[s] & 3) + 32 * (pc[s] >> 2) + (j)) * F_)
    unsigned xrd[4];
#pragma unroll
    for (int t = 0; t < 4; ++t)
      xrd[t] = w * 32768 + ((t + w) & 3) * 2048 + (i >> 3) * 1024 + (i & 7) * 128 + ((g ^ ((i >> 1) & 7)) * 16);
    v4u xf[2][4];
#define LDXF(s, t) xf[(s) & 1][t] = *(const v4u*)(smem + (xrd[t] ^ (unsigned)((pc[s] & 3) * 8192 + (pc[s] >> 2) * 64)))

#pragma unroll
    for (int R = 0; R < 8; ++R) LDXD(0, R);
#pragma unroll
    for (int R = 0; R < 8; ++R) LDXD(1, R);
    SB();
#pragma unroll
    for (int j = 0; j < 8; ++j) LDW(0, j);
    SB();
#pragma unroll
    for (int R = 0; R < 8; ++R) LDXD(2, R);
    SB();
#pragma unroll
    for (int j = 0; j < 8; ++j) LDW(1, j);
    SB();
    STAMP(1)
    f32x4 acc[4][8];
#pragma unroll
    for (int t = 0; t < 4; ++t)
#pragma unroll
      for (int c = 0; c < 8; ++c) acc[t][c] = f32x4{0.f, 0.f, 0.f, 0.f};
    VMWAIT(32);
#pragma unroll
    for (int t = 0; t < 4; ++t) LDXF(0, t);
    SB();

#pragma unroll
    for (int s = 0; s < 8; ++s) {
      if (s < 2) { VMWAIT(16); } else if (s == 2) { VMWAIT(8); }
      SB();
#pragma unroll
      for (int c = 0; c < 8; ++c) {
        if (s == 0) {
          if (c < 4) { LDXD(3, 2 * c); LDXD(3, 2 * c + 1); }
          else { LDW(2, 2 * (c - 4)); LDW(2, 2 * (c - 4) + 1); }
        } else if (s + 2 < 8) {
          LDW(s + 2, c);
        } else {
          LDW2(s - 6, c);
        }
        if (s + 1 < 8 && c < 4) LDXF(s + 1, c);
        const v4u f = colfrag(wb[s % 3], c);
#pragma unroll
        for (int t = 0; t < 4; ++t) acc[t][c] = mfma16(xf[s & 1][t], f, acc[t][c]);
        SB();
      }
      STAMP(2 + s)
    }

    f32x4* red = (f32x4*)smem;
#pragma unroll
    for (int t = 1; t < 4; ++t) {
#pragma unroll
      for (int c = 0; c < 8; ++c) {
        red[w * 2048 + ((t - 1) * 8 + c) * 64 + lane] = acc[t][c];
        if (c & 1) LDW2(2 + (((t - 1) * 4 + (c >> 1)) >> 3), ((t - 1) * 4 + (c >> 1)) & 7);
        SB();
      }
    }
    BAR();
    f32x4 fin[8];
#pragma unroll
    for (int c = 0; c < 8; ++c) {
      f32x4 sum = acc[0][c];
#pragma unroll
      for (int t = 1; t < 4; ++t) sum += red[((w - t) & 3) * 2048 + ((t - 1) * 8 + c) * 64 + lane];
      fin[c] = sum;
      if (c < 4) LDW2(3, 4 + c); else LDW2(4, c - 4);
      SB();
    }
    STAMP(10)
    const bool lo = i < 8;
    unsigned hv[2][4];
#pragma unroll
    for (int rr = 0; rr < 2; ++rr) {
      float h[8];
#pragma unroll
      for (int c = 0; c < 8; ++c) {
        const float send = lo ? fin[c][2 + rr] : fin[c][rr];
        const float recv = __builtin_bit_cast(float, __builtin_amdgcn_update_dpp(0, __builtin_bit_cast(int, send), 0x128, 0xf, 0xf, false));
        const float gpre = lo ? fin[c][rr] : recv;
        const float upre = lo ? recv : fin[c][2 + rr];
        const float gb = bf16_round(gpre);
        const float sg = bf16_round(gb * __builtin_amdgcn_rcpf(1.0f + __expf(-gb)));
        const float ub = bf16_round(upre);
        h[c] = sg * ub;
        if (rr == 0) { if (c < 4) LDW2(4, 4 + c); else LDW2(5, c - 4); }
        else { if (c < 4) LDW2(5, 4 + c); }
        SB();
      }
#pragma unroll
      for (int d = 0; d < 4; ++d) hv[rr][d] = f2bf_bits(h[2 * d]) | (f2bf_bits(h[2 * d + 1]) << 16);
    }
    {
      const int mtile16 = mt * 4 + w, ks = 2 * nt + ((i & 7) >> 2);
      const int hb = (int)H_OFF + (mtile16 * KS2 + ks) * 1024;
#pragma unroll
      for (int rr = 0; rr < 2; ++rr) {
        const int row = 4 * g + (lo ? 0 : 2) + rr;
        __builtin_amdgcn_raw_buffer_store_b128(v4u{hv[rr][0], hv[rr][1], hv[rr][2], hv[rr][3]}, rs, hb + ((i & 3) * 16 + row) * 16, 0, 16);
      }
    }
    STAMP(11)
    VMWAIT(0);
    BAR();
    if (tid == 0) {
      const int sq = nt / 14;
      __hip_atomic_store((GAS u64*)(ws + F1_OFF + (mt * 4 + sq) * 128 + (nt - 14 * sq) * 8), ep + 1, RLX_AGENT);
    }
#pragma unroll
    for (int j = 0; j < 8; ++j) LDW2(6, j);
    SB();
  }
  int bid2 = bid;
  asm volatile("" : "+s"(bid2));
  if (bid2 >= 224) {
#pragma unroll
    for (int q = 0; q < 7; ++q)
#pragma unroll
      for (int j = 0; j < 8; ++j) LDW2(q, j);
    SB();
  }
  STAMP(12)
  const u64 tag = ep + 1;

  v4u wf[7][8];
#pragma unroll
  for (int q = 0; q < 7; ++q) {
#pragma unroll
    for (int c = 0; c < 8; ++c) {
      wf[q][c] = colfrag(wdb[q], c);
    }
#pragma unroll
    for (int c = 0; c < 8; ++c) asm volatile("" : "+v"(wf[q][c]));
  }
  if (w == 0) {
    GAS u64* fl = (GAS u64*)(ws + F1_OFF + ((mt2 >> 1) * 4 + s2) * 128) + (lane < 14 ? lane : 0);
    for (unsigned spins = 0; spins < SPIN_CAP; ++spins) {
      const u64 v = __hip_atomic_load(fl, RLX_AGENT);
      if (__all(v == tag)) break;
      __builtin_amdgcn_s_sleep(1);
    }
  }
  BAR();
  STAMP(13)
  v4u hb[7][2];
  {
    const int hbase = (int)H_OFF + ((2 * mt2) * KS2 + ks0) * 1024 + lane * 16;
#pragma unroll
    for (int q = 0; q < 7; ++q)
#pragma unroll
      for (int t = 0; t < 2; ++t)
        hb[q][t] = __builtin_amdgcn_raw_buffer_load_b128(rs, hbase, (t * KS2 + PQ(q)) * 1024, 16);
    SB();
  }
  f32x4 acc2[2][8];
#pragma unroll
  for (int t = 0; t < 2; ++t)
#pragma unroll
    for (int c = 0; c < 8; ++c) acc2[t][c] = f32x4{0.f, 0.f, 0.f, 0.f};
#pragma unroll
  for (int q = 0; q < 7; ++q) {
#pragma unroll
    for (int c = 0; c < 8; ++c)
#pragma unroll
      for (int t = 0; t < 2; ++t) acc2[t][c] = mfma16(hb[q][t], wf[q][c], acc2[t][c]);
    SB();
  }
  STAMP(14)
  f32x4* red2 = (f32x4*)smem;
#pragma unroll
  for (int o = 0; o < 4; ++o) {
    if (o != w) {
      const int slot = o < w ? o : o - 1;
#pragma unroll
      for (int cc = 0; cc < 4; ++cc) red2[((w * 3 + slot) * 4 + cc) * 64 + lane] = acc2[o >> 1][4 * (o & 1) + cc];
    }
  }
  BAR();
  f32x4 fin2[4];
#pragma unroll
  for (int cc = 0; cc < 4; ++cc) {
    f32x4 own = w == 0 ? acc2[0][cc] : w == 1 ? acc2[0][4 + cc] : w == 2 ? acc2[1][cc] : acc2[1][4 + cc];
    f32x4 sum = f32x4{0.f, 0.f, 0.f, 0.f};
#pragma unroll
    for (int ww = 0; ww < 4; ++ww) {
      if (ww == w) sum += own;
      else {
        const int slot = w < ww ? w : w - 1;
        sum += red2[((ww * 3 + slot) * 4 + cc) * 64 + lane];
      }
    }
    fin2[cc] = sum;
  }
  const int tile = mt2 * 8 + nt2;
  GAS u64* xfl = (GAS u64*)(ws + F2_OFF + tile * 128);
  if (w != s2) {
    const int so = (int)SLAB_OFF + ((tile * 4 + w) * 4 + s2) * 4096 + lane * 16;
#pragma unroll
    for (int cc = 0; cc < 4; ++cc)
      __builtin_amdgcn_raw_buffer_store_b128(__builtin_bit_cast(v4u, fin2[cc]), rs, so + cc * 1024, 0, 16);
    VMWAIT(0);
    if (lane == 0) __hip_atomic_store(xfl + w * 4 + s2, tag, RLX_AGENT);
    if (bid == 0 && w == 1) {
      bool ok = false;
      for (unsigned spins = 0; spins < SPIN_CAP; ++spins) {
        bool m = true;
#pragma unroll
        for (int src = 0; src < 4; ++src)
          m &= __hip_atomic_load((GAS u64*)(ws + F2_OFF + lane * 128) + ((src + 1) & 3) * 4 + src, RLX_AGENT) == tag;
        if (__all(m)) { ok = true; break; }
        __builtin_amdgcn_s_sleep(1);
      }
      if (ok && lane == 0) __hip_atomic_store((GAS u64*)(ws + EP_OFF), tag, RLX_AGENT);
    }
  } else {
    {
      GAS u64* fl = xfl + s2 * 4 + ((lane & 3) == s2 ? ((s2 + 1) & 3) : (lane & 3));
      for (unsigned spins = 0; spins < SPIN_CAP; ++spins) {
        const u64 v = __hip_atomic_load(fl, RLX_AGENT);
        if (__all(v == tag)) break;
        __builtin_amdgcn_s_sleep(1);
      }
    }
    asm volatile("" ::: "memory");
    const int so = (int)SLAB_OFF + ((tile * 4 + s2) * 4) * 4096 + lane * 16;
    v4u sl[3][4];
#pragma unroll
    for (int k = 0; k < 3; ++k) {
      const int src = (s2 + 1 + k) & 3;
#pragma unroll
      for (int cc = 0; cc < 4; ++cc)
        sl[k][cc] = __builtin_amdgcn_raw_buffer_load_b128(rs, so + cc * 1024, src * 4096, 16);
    }
    f32x4 o[4];
#pragma unroll
    for (int cc = 0; cc < 4; ++cc) {
      f32x4 a = fin2[cc];
#pragma unroll
      for (int k = 0; k < 3; ++k) a += __builtin_bit_cast(f32x4, sl[k][cc]);
      o[cc] = a;
    }
    const int t = w >> 1, c0 = 4 * (w & 1);
#pragma unroll
    for (int rr = 0; rr < 4; ++rr) {
      const int row = mt2 * 32 + 16 * t + 4 * g + rr;
      v2u v;
      v.x = f2bf_bits(o[0][rr]) | (f2bf_bits(o[1][rr]) << 16);
      v.y = f2bf_bits(o[2][rr]) | (f2bf_bits(o[3][rr]) << 16);
      *(v2u*)(out + (size_t)row * D_ + nt2 * 128 + 8 * i + c0) = v;
    }
  }
  STAMP(15)
}

extern "C" void kernel_launch(void* const* d_in, const int* in_sizes, int n_in, void* d_out, int out_size,
                              void* d_ws, size_t ws_size, hipStream_t stream) {
  k_fused<<<256, 256, 0, stream>>>((const unsigned short*)d_in[0], (const unsigned short*)d_in[1], (const unsigned short*)d_in[2],
                                   (const unsigned short*)d_in[3], (unsigned char*)d_ws, (unsigned short*)d_out);
}
```

```cpp
#define WAUX 0
#define PFD 8000
#include <hip/hip_runtime.h>
#include <hip/hip_bf16.h>
#include <stdint.h>

typedef unsigned v4u __attribute__((ext_vector_type(4)));
typedef unsigned v2u __attribute__((ext_vector_type(2)));
typedef float f32x4 __attribute__((ext_vector_type(4)));
typedef float f32x2 __attribute__((ext_vector_type(2)));
typedef short bf16x8 __attribute__((ext_vector_type(8)));
typedef __bf16 bf16x2_t __attribute__((ext_vector_type(2)));
typedef unsigned long long u64;
#define GAS __attribute__((address_space(1)))
#define LAS __attribute__((address_space(3)))
#define RLX_AGENT __ATOMIC_RELAXED, __HIP_MEMORY_SCOPE_AGENT
#define SB() __builtin_amdgcn_sched_barrier(0)
#define BAR() asm volatile("s_waitcnt lgkmcnt(0)\n\ts_barrier" ::: "memory")
#define VMWAIT(n) asm volatile("s_waitcnt vmcnt(" #n ")" ::: "memory")

#define STAMP(n)

#define MAP2 1
#define HLD 0
#define NPRE 12
#define PFX 1
#define NLATE 0
#define SAUX 16
#define LAUX 16
#define XNT ""
constexpr int M_ = 256, D_ = 1024, F_ = 3584;
constexpr int KS2 = F_ / 32;

constexpr size_t EP_OFF = 0;
constexpr size_t F2_OFF = 4096;
constexpr size_t F1_OFF = 16384;
constexpr size_t H_OFF = 32768;
constexpr size_t H_BYTES = (size_t)M_ * F_ * 2;
constexpr size_t SLAB_OFF = H_OFF + H_BYTES;
constexpr size_t SLAB_BYTES = (size_t)64 * 16 * 4096;
constexpr size_t WS_BYTES = SLAB_OFF + SLAB_BYTES;
constexpr unsigned SPIN_CAP = 1u << 18;

__device__ __forceinline__ f32x4 mfma16(v4u a, v4u b, f32x4 c) {
  return __builtin_amdgcn_mfma_f32_16x16x32_bf16(__builtin_bit_cast(bf16x8, a), __builtin_bit_cast(bf16x8, b), c, 0, 0, 0);
}
__device__ __forceinline__ unsigned cvt_pk(float lo, float hi) {
  f32x2 v = {lo, hi};
  return __builtin_bit_cast(unsigned, __builtin_convertvector(v, bf16x2_t));
}
__device__ __forceinline__ unsigned f2u(float f) { return __builtin_bit_cast(unsigned, f); }
__device__ __forceinline__ float u2f(unsigned u) { return __builtin_bit_cast(float, u); }
__device__ __forceinline__ float bf_lo(unsigned p) { return __builtin_bit_cast(float, p << 16); }
__device__ __forceinline__ float bf_hi(unsigned p) { return __builtin_bit_cast(float, p & 0xffff0000u); }
__device__ __forceinline__ unsigned perm1(const v4u (&w)[8], int c, int t) {
  return __builtin_amdgcn_perm(w[2 * t + 1][c >> 1], w[2 * t][c >> 1], (c & 1) ? 0x07060302u : 0x05040100u);
}
__device__ __forceinline__ v4u colfrag(const v4u (&w)[8], int c) {
  v4u f;
#pragma unroll
  for (int t = 0; t < 4; ++t) f[t] = perm1(w, c, t);
  return f;
}
__device__ __forceinline__ void glds16(unsigned voff, const void* sbase, unsigned ldst) {
  unsigned keep;
  asm volatile("s_mov_b32 %0, m0\n\ts_mov_b32 m0, %3\n\ts_nop 0\n\tglobal_load_lds_dwordx4 %1, %2" XNT "\n\ts_mov_b32 m0, %0"
               : "=&s"(keep) : "v"(voff), "s"(sbase), "s"(ldst) : "memory");
}
template <int W>
__device__ __forceinline__ void red2_write(f32x4* red2, const f32x4 (&acc2)[2][8], int lane) {
#pragma unroll
  for (int o = 0; o < 4; ++o)
    if (o != W) {
#pragma unroll
      for (int cc = 0; cc < 4; ++cc) red2[((o * 4 + W) * 4 + cc) * 64 + lane] = acc2[o >> 1][4 * (o & 1) + cc];
    }
}
template <int W>
__device__ __forceinline__ void red2_read(const f32x4* red2, const f32x4 (&acc2)[2][8], f32x4 (&fin2)[4], int lane) {
#pragma unroll
  for (int cc = 0; cc < 4; ++cc) {
    f32x4 sum = acc2[W >> 1][4 * (W & 1) + cc];
#pragma unroll
    for (int ww = 0; ww < 4; ++ww)
      if (ww != W) sum += red2[((W * 4 + ww) * 4 + cc) * 64 + lane];
    fin2[cc] = sum;
  }
}

__global__ void __launch_bounds__(256, 1)
k_fused(const unsigned short* __restrict__ x, const unsigned short* __restrict__ wg,
        const unsigned short* __restrict__ wu, const unsigned short* __restrict__ wd,
        unsigned char* __restrict__ ws, unsigned short* __restrict__ out) {
  __shared__ __attribute__((aligned(16))) unsigned char smem[131072];
  const int tid = threadIdx.x, lane = tid & 63, w = __builtin_amdgcn_readfirstlane(tid >> 6);
  const int i = lane & 15, g = lane >> 4;
  const int bid = blockIdx.x, xcd = bid & 7, r = bid >> 3;
#define CNT(x)
  STAMP(0)
  u64 ep;
  __shared__ u64 ep_sh;
  const __amdgpu_buffer_rsrc_t rs = __builtin_amdgcn_make_buffer_rsrc((void*)ws, 0, (int)WS_BYTES, 0x00020000);
  const __amdgpu_buffer_rsrc_t rd = __builtin_amdgcn_make_buffer_rsrc((void*)wd, 0, F_ * D_ * 2, 0x00020000);

  const int mt2 = r >> 2, nt2 = (xcd & 1) * 4 + (r & 3), s2 = xcd >> 1;
  const int ks0 = 28 * s2 + 7 * w;
  const int rot2 = mt2 == 7 ? 0 : mt2;
  const int wdv = ((8 * g) * D_ + nt2 * 128 + i * 8) * 2;
  int pq[7];
#pragma unroll
  for (int q = 0; q < 7; ++q) pq[q] = __builtin_amdgcn_readfirstlane(ks0 + ((q + rot2) % 7));
  v4u wdb[7][8];
#define LDW2(n) wdb[(n) >> 3][(n) & 7] = __builtin_amdgcn_raw_buffer_load_b128(rd, wdv, (32 * pq[(n) >> 3] + ((n) & 7)) * (D_ * 2), WAUX)

#define LDW2E(n) if ((n) < 56 - NLATE) LDW2(n);
  if (bid < 224) {
    const int mt = r & 3, nt = xcd * 7 + (r >> 2);
    const unsigned lds0 = (unsigned)(size_t)(LAS unsigned char*)smem;
    const unsigned xreg = lds0 + w * 32768;
    const int r8 = lane >> 3;
    unsigned xv[2];
#pragma unroll
    for (int par = 0; par < 2; ++par) xv[par] = ((8 * par + r8) * D_ + (((lane & 7) ^ ((par << 2) | (r8 >> 1))) * 8)) * 2;
    const unsigned short* xw = x + (size_t)(mt * 64) * D_ + 256 * w;
#define XP(j) ((mt + (j)) & 3)
#define LDXD(j, R) glds16(xv[(R) & 1], xw + (size_t)(16 * ((R) >> 1)) * D_ + 64 * XP(j), xreg + XP(j) * 8192 + (R) * 1024)
    int pc[8];
#pragma unroll
    for (int s = 0; s < 8; ++s) pc[s] = ((mt + (s >> 1)) & 3) + 4 * (s & 1);
    const unsigned short* wp = (i < 8 ? wg : wu) + (size_t)(256 * w + 8 * g) * F_ + nt * 64 + (i & 7) * 8;
    v4u wb[8][8];
#define LDW(s, j) wb[s][j] = *(const v4u*)(wp + (size_t)(64 * (pc[s] & 3) + 32 * (pc[s] >> 2) + (j)) * F_)
#define LDW8(s) { _Pragma("unroll") for (int j = 0; j < 8; ++j) LDW(s, j); SB(); }
#define LDX8(p) { _Pragma("unroll") for (int R = 0; R < 8; ++R) LDXD(p, R); SB(); }
    unsigned xrd[4];
#pragma unroll
    for (int t = 0; t < 4; ++t)
      xrd[t] = w * 32768 + ((t + w) & 3) * 2048 + (i >> 3) * 1024 + (i & 7) * 128 + ((g ^ ((i >> 1) & 7)) * 16);
    v4u xf[2][4];
#define LDXF(s, t) xf[(s) & 1][t] = *(const v4u*)(smem + (xrd[t] ^ (unsigned)((pc[s] & 3) * 8192 + (pc[s] >> 2) * 64)))

    LDW8(0) LDW8(1) LDX8(0) LDW8(2) LDX8(1)
    STAMP(1)
    f32x4 acc[4][8];
#pragma unroll
    for (int t = 0; t < 4; ++t)
#pragma unroll
      for (int c = 0; c < 8; ++c) acc[t][c] = f32x4{0.f, 0.f, 0.f, 0.f};
    VMWAIT(16);
#pragma unroll
    for (int t = 0; t < 4; ++t) LDXF(0, t);
    SB();
    v4u fcur = colfrag(wb[0], 0);
    f32x4* red = (f32x4*)smem;

#pragma unroll
    for (int s = 0; s < 8; ++s) {
      if (s == 1 || s == 5) { VMWAIT(16); } else if (s == 3) { VMWAIT(24); }
      SB();
#pragma unroll
      for (int c = 0; c < 8; ++c) {
        const bool last = (s == 7 && c == 7);
        v4u fn;
        acc[0][c] = mfma16(xf[s & 1][0], fcur, acc[0][c]);
        SB();
        if (!last) { fn[0] = perm1(wb[c < 7 ? s : s + 1], (c + 1) & 7, 0); fn[1] = perm1(wb[c < 7 ? s : s + 1], (c + 1) & 7, 1); }
        SB();
        acc[1][c] = mfma16(xf[s & 1][1], fcur, acc[1][c]);
        SB();
        if (!last) { fn[2] = perm1(wb[c < 7 ? s : s + 1], (c + 1) & 7, 2); fn[3] = perm1(wb[c < 7 ? s : s + 1], (c + 1) & 7, 3); }
        SB();
        acc[2][c] = mfma16(xf[s & 1][2], fcur, acc[2][c]);
        SB();
        if (s < 5) { LDW(s + 3, c); if (s == 0) LDXD(2, c); if (s == 2) LDXD(3, c); }
        else LDW2((s - 5) * 8 + c);
        if (s == 5 && c == 0 && w == 0) ep = __hip_atomic_load((GAS u64*)(ws + EP_OFF), RLX_AGENT);
        SB();
        acc[3][c] = mfma16(xf[s & 1][3], fcur, acc[3][c]);
        SB();
        if (s + 1 < 8 && c < 4) LDXF(s + 1, c);
        if (s == 7 && c > 0) {
#pragma unroll
          for (int t = 1; t < 4; ++t) red[w * 2048 + ((t - 1) * 8 + c - 1) * 64 + lane] = acc[t][c - 1];
        }
        SB();
        if (!last) fcur = fn;
      }
      STAMP(2 + s)
    }
#pragma unroll
    for (int t = 1; t < 4; ++t) red[w * 2048 + ((t - 1) * 8 + 7) * 64 + lane] = acc[t][7];
    if (w == 0) ep_sh = ep;
    SB();
#pragma unroll
    for (int n = 24; n < 24 + NPRE; ++n) LDW2(n);
    SB();
    BAR();
    ep = ep_sh;
    f32x4 fin[8];
#pragma unroll
    for (int c = 0; c < 8; ++c) {
      f32x4 sum = acc[0][c];
#pragma unroll
      for (int t = 1; t < 4; ++t) sum += red[((w - t) & 3) * 2048 + ((t - 1) * 8 + c) * 64 + lane];
      fin[c] = sum;
    }
    STAMP(10)
    float ga[8][2], ua[8][2];
#pragma unroll
    for (int c = 0; c < 8; ++c)
#pragma unroll
      for (int rr = 0; rr < 2; ++rr) {
        const int A = (int)f2u(fin[c][rr]), B = (int)f2u(fin[c][2 + rr]);
        ua[c][rr] = __builtin_bit_cast(float, __builtin_amdgcn_update_dpp(B, A, 0x128, 0xf, 0x3, false));
        ga[c][rr] = __builtin_bit_cast(float, __builtin_amdgcn_update_dpp(A, B, 0x128, 0xf, 0xc, false));
      }
    SB();
    float gb[8][2], e[8][2];
#pragma unroll
    for (int c = 0; c < 8; ++c) {
      const unsigned p = cvt_pk(ga[c][0], ga[c][1]);
      gb[c][0] = bf_lo(p); gb[c][1] = bf_hi(p);
    }
    SB();
#pragma unroll
    for (int c = 0; c < 8; ++c)
#pragma unroll
      for (int rr = 0; rr < 2; ++rr) e[c][rr] = __expf(-gb[c][rr]);
    SB();
#pragma unroll
    for (int c = 0; c < 8; ++c)
#pragma unroll
      for (int rr = 0; rr < 2; ++rr) e[c][rr] = __builtin_amdgcn_rcpf(1.0f + e[c][rr]);
    SB();
    float hh[8][2];
#pragma unroll
    for (int c = 0; c < 8; ++c) {
      const unsigned ps = cvt_pk(gb[c][0] * e[c][0], gb[c][1] * e[c][1]);
      const unsigned pu = cvt_pk(ua[c][0], ua[c][1]);
      hh[c][0] = bf_lo(ps) * bf_lo(pu);
      hh[c][1] = bf_hi(ps) * bf_hi(pu);
    }
    {
      const bool lo = i < 8;
      const int mtile16 = mt * 4 + w;
      unsigned char* tb = smem + w * 32768 + 24576;
      const int wa = ((i & 7) >> 2) * 1152 + (i & 3) * 272 + (4 * g + (lo ? 0 : 2)) * 16;
#pragma unroll
      for (int rr = 0; rr < 2; ++rr) {
        v4u v;
#pragma unroll
        for (int d = 0; d < 4; ++d) v[d] = cvt_pk(hh[2 * d][rr], hh[2 * d + 1][rr]);
        *(v4u*)(tb + wa + rr * 16) = v;
      }
      asm volatile("s_waitcnt lgkmcnt(0)" ::: "memory");
      const int hb = (int)H_OFF + (mtile16 * KS2 + 2 * nt) * 1024 + lane * 16;
#pragma unroll
      for (int kh = 0; kh < 2; ++kh) {
        const v4u v = *(const v4u*)(tb + kh * 1152 + g * 272 + i * 16);
        __builtin_amdgcn_raw_buffer_store_b128(v, rs, hb + kh * 1024, 0, SAUX);
      }
      STAMP(11)
      VMWAIT(0);
      if (lane == 0) __hip_atomic_store((GAS u64*)(ws + F1_OFF) + mtile16 * 64 + nt, ep + 1, RLX_AGENT);
    }
#pragma unroll
    for (int n = 24 + NPRE; n < 56; ++n) LDW2(n);
    SB();
  }
  int bid2 = bid;
  asm volatile("" : "+s"(bid2));
  if (bid2 >= 224) {
    {
      const __amdgpu_buffer_rsrc_t rx = __builtin_amdgcn_make_buffer_rsrc((void*)x, 0, M_ * D_ * 2, 0x00020000);
      unsigned tx[4];
#pragma unroll
      for (int t = 0; t < 4; ++t) tx[t] = __builtin_amdgcn_raw_buffer_load_b32(rx, (((r - 28) * 4 + w) * 256 + t * 64 + lane) * 128, 0, 0);
#pragma unroll
      for (int t = 0; t < 4; ++t) asm volatile("" :: "v"(tx[t]));
    }
    {
      const unsigned long long t0 = __builtin_amdgcn_s_memtime();
      while (__builtin_amdgcn_s_memtime() - t0 < PFD) __builtin_amdgcn_s_sleep(4);
      unsigned td[7];
#pragma unroll
      for (int t = 0; t < 7; ++t) {
        const int id = ((((r - 28) * 4 + w) * 7 + t) * 64 + lane);
        td[t] = __builtin_amdgcn_raw_buffer_load_b32(rd, (896 * s2 + (id >> 3)) * (D_ * 2) + (xcd & 1) * 1024 + (id & 7) * 128, 0, WAUX);
      }
#pragma unroll
      for (int t = 0; t < 7; ++t) asm volatile("" :: "v"(td[t]));
    }
#pragma unroll
    for (int n = 0; n < 56; ++n) LDW2(n);
    SB();
    if (w == 0) ep_sh = __hip_atomic_load((GAS u64*)(ws + EP_OFF), RLX_AGENT);
    BAR();
    ep = ep_sh;
  }
  const u64 tag = ep + 1;
  STAMP(12)

  v4u wf[7][8];
#pragma unroll
  for (int q = 0; q < 7; ++q) {
#pragma unroll
    for (int c = 0; c < 8; ++c) wf[q][c] = colfrag(wdb[q], c);
#pragma unroll
    for (int c = 0; c < 8; ++c) asm volatile("" : "+v"(wf[q][c]));
  }
  {
    GAS u64* fl = (GAS u64*)(ws + F1_OFF) + (2 * mt2 + ((lane >> 2) & 1)) * 64 + (ks0 >> 1) + (lane & 3);
    for (unsigned spins = 0; spins < SPIN_CAP; ++spins) {
      const u64 v = __hip_atomic_load(fl, RLX_AGENT);
      CNT(cnt_poll);
      if (__all(v == tag)) break;
      __builtin_amdgcn_s_sleep(1);
    }
  }
  asm volatile("" ::: "memory");
#define HAUX LAUX
  STAMP(13)
  f32x4 acc2[2][8];
  {
    const int hbase = (int)H_OFF + (2 * mt2) * KS2 * 1024 + lane * 16;
    v4u hr[7][2];
#pragma unroll
    for (int q = 0; q < 7; ++q)
#pragma unroll
      for (int t = 0; t < 2; ++t)
        hr[q][t] = __builtin_amdgcn_raw_buffer_load_b128(rs, hbase, (t * KS2 + pq[q]) * 1024, HAUX);
    SB();
#pragma unroll
    for (int t = 0; t < 2; ++t)
#pragma unroll
      for (int c = 0; c < 8; ++c) acc2[t][c] = f32x4{0.f, 0.f, 0.f, 0.f};
#pragma unroll
    for (int q = 0; q < 7; ++q)
#pragma unroll
      for (int c = 0; c < 8; ++c)
#pragma unroll
        for (int t = 0; t < 2; ++t) acc2[t][c] = mfma16(hr[q][t], wf[q][c], acc2[t][c]);
  }
  STAMP(14)
  f32x4* red2 = (f32x4*)smem;
  BAR();
  if (w == 0) red2_write<0>(red2, acc2, lane); else if (w == 1) red2_write<1>(red2, acc2, lane);
  else if (w == 2) red2_write<2>(red2, acc2, lane); else red2_write<3>(red2, acc2, lane);
  BAR();
  f32x4 fin2[4];
  if (w == 0) red2_read<0>(red2, acc2, fin2, lane); else if (w == 1) red2_read<1>(red2, acc2, fin2, lane);
  else if (w == 2) red2_read<2>(red2, acc2, fin2, lane); else red2_read<3>(red2, acc2, fin2, lane);

  const int tile = mt2 * 8 + nt2;
  GAS u64* xfl = (GAS u64*)(ws + F2_OFF + tile * 128);
  if (w != s2) {
    const int so = (int)SLAB_OFF + ((tile * 4 + w) * 4 + s2) * 4096 + lane * 16;
#pragma unroll
    for (int cc = 0; cc < 4; ++cc)
      __builtin_amdgcn_raw_buffer_store_b128(__builtin_bit_cast(v4u, fin2[cc]), rs, so + cc * 1024, 0, SAUX);
    VMWAIT(0);
    if (lane == 0) __hip_atomic_store(xfl + w * 4 + s2, tag, RLX_AGENT);
    if (bid == 0 && w == 1) {
      bool ok = false;
      for (unsigned spins = 0; spins < SPIN_CAP; ++spins) {
        bool m = true;
#pragma unroll
        for (int src = 0; src < 4; ++src)
          m &= __hip_atomic_load((GAS u64*)(ws + F2_OFF + lane * 128) + ((src + 1) & 3) * 4 + src, RLX_AGENT) == tag;
        if (__all(m)) { ok = true; break; }
        __builtin_amdgcn_s_sleep(1);
      }
      if (ok && lane == 0) __hip_atomic_store((GAS u64*)(ws + EP_OFF), tag, RLX_AGENT);
    }
  } else {
    {
      GAS u64* fl = xfl + s2 * 4 + ((lane & 3) == s2 ? ((s2 + 1) & 3) : (lane & 3));
      for (unsigned spins = 0; spins < SPIN_CAP; ++spins) {
        const u64 v = __hip_atomic_load(fl, RLX_AGENT);
        CNT(cnt_x);
        if (__all(v == tag)) break;
        __builtin_amdgcn_s_sleep(1);
      }
    }
    asm volatile("" ::: "memory");
    const int so = (int)SLAB_OFF + ((tile * 4 + s2) * 4) * 4096 + lane * 16;
    v4u sl[3][4];
#pragma unroll
    for (int k = 0; k < 3; ++k) {
      const int src = (s2 + 1 + k) & 3;
#pragma unroll
      for (int cc = 0; cc < 4; ++cc)
        sl[k][cc] = __builtin_amdgcn_raw_buffer_load_b128(rs, so + cc * 1024, src * 4096, LAUX);
    }
    f32x4 o[4];
#pragma unroll
    for (int cc = 0; cc < 4; ++cc) {
      f32x4 a = fin2[cc];
#pragma unroll
      for (int k = 0; k < 3; ++k) a += __builtin_bit_cast(f32x4, sl[k][cc]);
      o[cc] = a;
    }
    const int t = w >> 1, c0 = 4 * (w & 1);
#pragma unroll
    for (int rr = 0; rr < 4; ++rr) {
      const int row = mt2 * 32 + 16 * t + 4 * g + rr;
      v2u v;
      v.x = cvt_pk(o[0][rr], o[1][rr]);
      v.y = cvt_pk(o[2][rr], o[3][rr]);
      *(v2u*)(out + (size_t)row * D_ + nt2 * 128 + 8 * i + c0) = v;
    }
  }
  STAMP(15)
}

extern "C" void kernel_launch(void* const* d_in, const int* in_sizes, int n_in, void* d_out, int out_size,
                              void* d_ws, size_t ws_size, hipStream_t stream) {
  k_fused<<<256, 256, 0, stream>>>((const unsigned short*)d_in[0], (const unsigned short*)d_in[1], (const unsigned short*)d_in[2],
                                   (const unsigned short*)d_in[3], (unsigned char*)d_ws, (unsigned short*)d_out);
}
```

```cpp
#include <hip/hip_runtime.h>
#include <hip/hip_bf16.h>
#include <stdint.h>

typedef unsigned v4u __attribute__((ext_vector_type(4)));
typedef unsigned v2u __attribute__((ext_vector_type(2)));
typedef float f32x4 __attribute__((ext_vector_type(4)));
typedef float f32x2 __attribute__((ext_vector_type(2)));
typedef short bf16x8 __attribute__((ext_vector_type(8)));
typedef __bf16 bf16x2_t __attribute__((ext_vector_type(2)));
typedef unsigned long long u64;
#define GAS __attribute__((address_space(1)))
#define LAS __attribute__((address_space(3)))
#define RLX_AGENT __ATOMIC_RELAXED, __HIP_MEMORY_SCOPE_AGENT
#define SB() __builtin_amdgcn_sched_barrier(0)
#define BAR() asm volatile("s_waitcnt lgkmcnt(0)\n\ts_barrier" ::: "memory")
#define BARN(n) asm volatile("s_waitcnt lgkmcnt(" #n ")\n\ts_barrier" ::: "memory")
#define VMWAIT(n) asm volatile("s_waitcnt vmcnt(" #n ")" ::: "memory")

#define STAMP(n)

#define TST(n)
#define FSTAMP(n)
#define FSTAMP2(n, v)
#define XSTAMP(n)
#define XSTAMP3(n, a, b, c)
#define XCNT(c)
#define XDRAIN()
#define TST2(n) TST(n)
constexpr int M_ = 256, D_ = 1024, F_ = 3584;
constexpr int KS2 = F_ / 32;

constexpr size_t EP_OFF = 0;
constexpr size_t AF_OFF = 4096;
constexpr size_t AF2_OFF = 16384;
constexpr int HN_D = 16384;
constexpr size_t XF_OFF = 32768;
constexpr size_t H_OFF = 65536;
constexpr size_t H_BYTES = (size_t)M_ * F_ * 2;
constexpr size_t SLAB_OFF = H_OFF + H_BYTES;
constexpr size_t SLAB_BYTES = (size_t)64 * 16 * 4096;
constexpr size_t WS_BYTES = SLAB_OFF + SLAB_BYTES;
constexpr unsigned SPIN_CAP = 1u << 16;
constexpr int SC1 = 16;
constexpr unsigned SENTW = 0xFFFFFFFFu;

__device__ __forceinline__ f32x4 mfma16(v4u a, v4u b, f32x4 c) {
  return __builtin_amdgcn_mfma_f32_16x16x32_bf16(__builtin_bit_cast(bf16x8, a), __builtin_bit_cast(bf16x8, b), c, 0, 0, 0);
}
__device__ __forceinline__ unsigned cvt_pk(float lo, float hi) {
  f32x2 v = {lo, hi};
  return __builtin_bit_cast(unsigned, __builtin_convertvector(v, bf16x2_t));
}
__device__ __forceinline__ unsigned f2u(float f) { return __builtin_bit_cast(unsigned, f); }
__device__ __forceinline__ float u2f(unsigned u) { return __builtin_bit_cast(float, u); }
__device__ __forceinline__ float bf_lo(unsigned p) { return __builtin_bit_cast(float, p << 16); }
__device__ __forceinline__ float bf_hi(unsigned p) { return __builtin_bit_cast(float, p & 0xffff0000u); }
__device__ __forceinline__ unsigned perm1(const v4u (&w)[8], int c, int t) {
  return __builtin_amdgcn_perm(w[2 * t + 1][c >> 1], w[2 * t][c >> 1], (c & 1) ? 0x07060302u : 0x05040100u);
}
__device__ __forceinline__ v4u colfrag(const v4u (&w)[8], int c) {
  v4u f;
#pragma unroll
  for (int t = 0; t < 4; ++t) f[t] = perm1(w, c, t);
  return f;
}
#define MFMAV(acc, a, b) asm volatile("v_mfma_f32_16x16x32_bf16 %0, %1, %2, %0" : "+v"(acc) : "v"(a), "v"(b))
#define MFMAV0(acc, a, b) asm volatile("v_mfma_f32_16x16x32_bf16 %0, %1, %2, 0" : "=v"(acc) : "v"(a), "v"(b))
__device__ __forceinline__ unsigned umax4(v4u v) { return max(max(v[0], v[1]), max(v[2], v[3])); }
__device__ __forceinline__ v4u pack24(float a, float b, float c, float d, unsigned t) {
  const unsigned ua = f2u(a) + 0x80u, ub = f2u(b) + 0x80u, uc = f2u(c) + 0x80u, ud = f2u(d) + 0x80u;
  return v4u{__builtin_amdgcn_perm(ua, ub, 0x07060501u), __builtin_amdgcn_perm(ub, t, 0x07060100u),
             __builtin_amdgcn_perm(uc, ud, 0x07060501u), __builtin_amdgcn_perm(ud, t, 0x07060302u)};
}
__device__ __forceinline__ unsigned tag24(v4u v) { return __builtin_amdgcn_perm(v[3], v[1], 0x05040100u); }
__device__ __forceinline__ f32x4 unpack24(v4u v) {
  const v4u r = {v[0] & 0xffffff00u, __builtin_amdgcn_perm(v[1], v[0], 0x0706000cu), v[2] & 0xffffff00u, __builtin_amdgcn_perm(v[3], v[2], 0x0706000cu)};
  return __builtin_bit_cast(f32x4, r);
}
__device__ __forceinline__ unsigned clampw(unsigned v) { return min(v, SENTW - 1u); }

__global__ void __launch_bounds__(256, 1)
k_fused(const unsigned short* x, const unsigned short* wg, const unsigned short* wu, const unsigned short* wd,
        unsigned char* ws, unsigned short* out) {
  __shared__ __attribute__((aligned(16))) unsigned char smem[163840];
  const int tid = threadIdx.x, lane = tid & 63, w = __builtin_amdgcn_readfirstlane(tid >> 6);
  const int i = lane & 15, g = lane >> 4;
  const int bid = blockIdx.x, xcd = bid & 7, r = bid >> 3;
#define CNT(x)
  STAMP(0)
  const v4u SENT = {SENTW, SENTW, SENTW, SENTW};
  const int s2 = xcd >> 1;

#define COMMONA() \
  const int mt2 = r >> 2, nt2 = (xcd & 1) * 4 + (r & 3); \
  const int tile = mt2 * 8 + nt2; \
  const int ks0 = 28 * s2 + 7 * w; \
  int wdv = ((32 * (g & 1) + 16 * (g >> 1)) * D_ + nt2 * 128 + i * 8) * 2; \
  int so_w = (int)SLAB_OFF + w * 1024 + lane * 16; \
  int dso[7], hso[7], xin[3], xo[3], xout; \
  unsigned qmp = 0; \
  {   \
    const unsigned q4 = (0x4206420u >> (4 * (mt2 & 3))) & 0xffffu, q3 = (0x31531u >> (4 * ((0x4924 >> (2 * mt2)) & 3))) & 0xfffu; \
    unsigned pk = (w & 1) ? (q3 | q4 << 12) : (q4 | q3 << 16); \
    if (0) { \
      unsigned gd = 0, bd = 0; int ng = 0, nb = 0; \
      _Pragma("unroll") for (int q = 0; q < 7; ++q) { \
        const unsigned l = (pk >> (4 * q)) & 7; \
        if (((ks0 + l) & 14) == 2 * 3) { bd |= l << nb; nb += 4; } else { gd |= l << ng; ng += 4; } } \
      pk = gd | bd << ng; } \
    _Pragma("unroll") for (int q = 0; q < 7; ++q) { \
      const int p = ks0 + ((pk >> (4 * q)) & 7); \
      hso[q] = p * 1024; dso[q] = (64 * (p >> 1) + 4 * (p & 1)) * (D_ * 2); \
      qmp |= 1u << (8 * ((p >> 1) - (ks0 >> 1)) + q); \
      asm volatile("" : "+s"(hso[q]), "+s"(dso[q])); } \
    if (0) asm volatile("" : "+s"(qmp)); \
    _Pragma("unroll") for (int k = 0; k < 3; ++k) { \
      xin[k] = ((tile * 4 + s2) * 4 + (1 ? s2 ^ (k + 1) : (s2 + 1 + k) & 3)) * 4096; asm volatile("" : "+s"(xin[k])); \
      xo[k] = ((tile * 4 + (s2 ^ (k + 1))) * 4 + s2) * 4096; if (1) asm volatile("" : "+s"(xo[k])); } \
    xout = (tile * 16 + s2) * 4096; if (!1) asm volatile("" : "+s"(xout)); } \
    \
  int gfo, gfo2 = 0; \
  if (1) { gfo2 = (int)AF2_OFF + (2 * tile + (lane & 1)) * 8; asm volatile("" : "+v"(gfo2)); } \
  if (1 == 1) {   \
    const int uw = ((r & 3) * 4 + w) * 8 + xcd, ur = (2 * mt2 + (lane & 1)) * 8 + 2 * s2 + (w >> 1); \
    gfo = (int)AF_OFF + (896 + ((lane == 1 || lane == 2) ? ur : uw)) * 8; \
  } else { const int m16 = 2 * mt2 + ((lane >> 2) & 1), pnt = (ks0 >> 1) + (lane & 3); \
    const int pb = ((pnt % 7) * 4 + (m16 >> 2)) * 8 + pnt / 7; \
    const int l3 = lane < 11 ? lane - 8 : 0; \
    const int db = r * 8 + 2 * ((s2 + 1 + l3) & 3) + (xcd & 1); \
    int ga = pb * 4 + (m16 & 3), gb = db * 4 + w; \
    if (1) asm volatile("" : "+v"(ga), "+v"(gb)); \
    gfo = (int)AF_OFF + (lane < 8 ? ga : gb) * 8; \
      \
    if ((0 || 0) && (lane & 56) == 16) gfo = (int)H_OFF + (m16 * KS2 + 2 * pnt + 0) * 1024 + 1016; } \
  int hbase = (int)H_OFF + (2 * mt2 + (0 ? w >> 1 : 0)) * KS2 * 1024 + (1 ? lane ^ s2 : lane) * 16; \
  int oo = ((mt2 * 32 + 16 * (w >> 1) + 4 * g + s2) * D_ + nt2 * 128 + 8 * i + 4 * (w & 1)) * 2; \
  int fo = lane == 0 ? (int)AF_OFF + (bid * 4 + w) * 8 : 0x7ffffff0; \
  asm volatile("" : "+v"(wdv), "+v"(so_w), "+v"(gfo), "+v"(hbase), "+v"(oo), "+v"(fo));
#define COMMONB() \
  const __amdgpu_buffer_rsrc_t rs = __builtin_amdgcn_make_buffer_rsrc((void*)ws, 0, (int)WS_BYTES, 0x00020000); \
  const __amdgpu_buffer_rsrc_t rd = __builtin_amdgcn_make_buffer_rsrc((void*)wd, 0, F_ * D_ * 2, 0x00020000); \
  v4u wdb[7][8], hr[7][2]; \
  v2u gv, gv2, tgv; \
  int wd4[4];
#define COMMON() COMMONB()
#define PRODA() \
    int hb = (int)H_OFF + ((mt * 4 + w) * KS2 + 2 * nt) * 1024 + lane * 16; \
    unsigned xrd = w * 32768 + (i >> 3) * 1024 + (i & 7) * 128 + ((g ^ ((i >> 1) & 7)) * 16); \
    unsigned rdo[3]; \
    _Pragma("unroll") for (int t = 1; t < 4; ++t) rdo[t - 1] = ((w - t) & 3) * 32768 + (t - 1) * 8192; \
    asm volatile("" : "+v"(hb), "+v"(xrd), "+s"(rdo[0]), "+s"(rdo[1]), "+s"(rdo[2])); \
    asm volatile("" : "+s"(wso[3]), "+s"(wso[4]), "+s"(wso[5]), "+s"(wso[6]), "+s"(wso[7])); \
    asm volatile("" : "+s"(xso[0]), "+s"(xso[1]), "+s"(xso[2]), "+s"(xso[3]), "+s"(xso[4]), "+s"(xso[5]), "+s"(xso[6]), "+s"(xso[7]));
#define LDT() tgv = __builtin_amdgcn_raw_buffer_load_b64(rs, 0, (int)EP_OFF, 0)
#define GETT() tag = (u64)(unsigned)__builtin_amdgcn_readfirstlane(tgv[0]) | (u64)(unsigned)__builtin_amdgcn_readfirstlane(tgv[1]) << 32
#define PINTAG() { asm volatile("" : "+s"(tag)); tag += 1; }
#define FLAG() { const v2u tv = {(unsigned)tag, (unsigned)(tag >> 32)}; __builtin_amdgcn_raw_buffer_store_b64(tv, rs, fo, 0, SC1); }
#define HINT() { const v2u tv = {(unsigned)tag, (unsigned)__builtin_amdgcn_s_memrealtime()}; __builtin_amdgcn_raw_buffer_store_b64(tv, rs, fo, HN_D, SC1); }
#define XANN() { const v2u tv = {(unsigned)tag, (unsigned)(tag >> 32)}; __builtin_amdgcn_raw_buffer_store_b64(tv, rs, xfo, 0, SC1); }
#define FLAGA() { const v2u tv = {(unsigned)tag, (unsigned)(tag >> 32)}; __builtin_amdgcn_raw_buffer_store_b64(tv, rs, xfo, 64, SC1); }
#define AOK(v) ((__ballot((v[0] == (unsigned)tag) & (v[1] == (unsigned)(tag >> 32))) | 0x0000ffff0000ffffull) == ~0ull)
#define LDXG() xg = __builtin_amdgcn_raw_buffer_load_b64(rs, xfi, 0, SC1)
#define XGOK() (__ballot((xg[0] == (unsigned)tag) & (xg[1] == (unsigned)(tag >> 32))) == ~0ull)
#define XGOK1() (__ballot((xg1[0] == (unsigned)tag) & (xg1[1] == (unsigned)(tag >> 32))) == ~0ull)
#define XANNT() { const v2u tv = {(unsigned)tag, xrt}; __builtin_amdgcn_raw_buffer_store_b64(tv, rs, xfo, 0, SC1); }
#define XTOK() __all(xg[0] == (unsigned)tag)
#define XTOK1() __all(xg1[0] == (unsigned)tag)
#define WD4() { wd4[0] = wdv; wd4[1] = wdv + 2 * D_ * 2; wd4[2] = wdv + 8 * D_ * 2; wd4[3] = wdv + 10 * D_ * 2; \
    asm volatile("" : "+v"(wd4[1]), "+v"(wd4[2]), "+v"(wd4[3])); }
#define LDW2(n) wdb[(n) >> 3][(n) & 7] = __builtin_amdgcn_raw_buffer_load_b128(rd, wd4[((n) & 7) >> 1] + ((n) & 1) * (D_ * 2), dso[(n) >> 3], 0)
#define ARMS(k) __builtin_amdgcn_raw_buffer_store_b128(SENT, rs, so_w, xin[k], SC1)
#define LDG() gv = __builtin_amdgcn_raw_buffer_load_b64(rs, gfo, 0, SC1)
#define LDG2() gv2 = __builtin_amdgcn_raw_buffer_load_b64(rs, gfo2, 0, SC1)
#define GOK2() (__ballot((gv2[0] == (unsigned)tag) & (gv2[1] == (unsigned)(tag >> 32))) == ~0ull)
#define GWAIT2() { _Pragma("nounroll") for (unsigned spins = 0;; ++spins) { \
    __builtin_amdgcn_s_sleep(1); asm volatile("" ::: "memory"); LDG2(); CNT(cnt_poll); \
    if (GOK2() || spins >= SPIN_CAP) break; } asm volatile("" ::: "memory"); }
#define GOK() ((__ballot((gv[0] == (unsigned)tag) & (gv[1] == (unsigned)(tag >> 32))) | ((0 || 0) ? 0xff0000ull : 0ull)) == ~0ull)
#define EARLYH() { const unsigned nr = (unsigned)(__ballot(gv[1] != SENTW) >> 16); unsigned rb = nr & (nr >> 4) & 15u; \
    if (!GOK()) rb = 0; \
    em = ((rb & 1) ? qmp & 127u : 0u) | ((rb & 2) ? (qmp >> 8) & 127u : 0u) | ((rb & 4) ? (qmp >> 16) & 127u : 0u) | ((rb & 8) ? qmp >> 24 : 0u); \
    asm volatile("" : "+s"(em) :: "memory"); \
    if (em) { asm volatile("s_bitcmp0_b32 %[m], 0\n\ts_cbranch_scc1 1f\n\tbuffer_load_dwordx4 a[200:203], %[b0], %[rs], %[o0] offen sc1\n\tbuffer_load_dwordx4 a[204:207], %[b1], %[rs], %[o0] offen sc1\n1:\n\ts_bitcmp0_b32 %[m], 1\n\ts_cbranch_scc1 2f\n\tbuffer_load_dwordx4 a[208:211], %[b0], %[rs], %[o1] offen sc1\n\tbuffer_load_dwordx4 a[212:215], %[b1], %[rs], %[o1] offen sc1\n2:\n\ts_bitcmp0_b32 %[m], 2\n\ts_cbranch_scc1 3f\n\tbuffer_load_dwordx4 a[216:219], %[b0], %[rs], %[o2] offen sc1\n\tbuffer_load_dwordx4 a[220:223], %[b1], %[rs], %[o2] offen sc1\n3:\n\ts_bitcmp0_b32 %[m], 3\n\ts_cbranch_scc1 4f\n\tbuffer_load_dwordx4 a[224:227], %[b0], %[rs], %[o3] offen sc1\n\tbuffer_load_dwordx4 a[228:231], %[b1], %[rs], %[o3] offen sc1\n4:\n\ts_bitcmp0_b32 %[m], 4\n\ts_cbranch_scc1 5f\n\tbuffer_load_dwordx4 a[232:235], %[b0], %[rs], %[o4] offen sc1\n\tbuffer_load_dwordx4 a[236:239], %[b1], %[rs], %[o4] offen sc1\n5:\n\ts_bitcmp0_b32 %[m], 5\n\ts_cbranch_scc1 6f\n\tbuffer_load_dwordx4 a[240:243], %[b0], %[rs], %[o5] offen sc1\n\tbuffer_load_dwordx4 a[244:247], %[b1], %[rs], %[o5] offen sc1\n6:\n\ts_bitcmp0_b32 %[m], 6\n\ts_cbranch_scc1 7f\n\tbuffer_load_dwordx4 a[248:251], %[b0], %[rs], %[o6] offen sc1\n\tbuffer_load_dwordx4 a[252:255], %[b1], %[rs], %[o6] offen sc1\n7:\n\t" :: [b0] "v"(hbase), [b1] "v"(hbase1), [rs] "s"(rs), [m] "s"(em), [o0] "s"(hso[0]), [o1] "s"(hso[1]), [o2] "s"(hso[2]), [o3] "s"(hso[3]), [o4] "s"(hso[4]), [o5] "s"(hso[5]), [o6] "s"(hso[6]) : "memory", "scc", "a200", "a201", "a202", "a203", "a204", "a205", "a206", "a207", "a208", "a209", "a210", "a211", "a212", "a213", "a214", "a215", "a216", "a217", "a218", "a219", "a220", "a221", "a222", "a223", "a224", "a225", "a226", "a227", "a228", "a229", "a230", "a231", "a232", "a233", "a234", "a235", "a236", "a237", "a238", "a239", "a240", "a241", "a242", "a243", "a244", "a245", "a246", "a247", "a248", "a249", "a250", "a251", "a252", "a253", "a254", "a255"); } }
#define HWAIT(n, ...) asm volatile("s_waitcnt vmcnt(" #n ")" : __VA_ARGS__ :: "memory");
#define HPIN(q)
#define XBAD(k) __any(umax4(sl[k]) == SENTW)
#define LDH(q) { hr[q][0] = __builtin_amdgcn_raw_buffer_load_b128(rs, hbase, hso[q], SC1); SB(); \
    hr[q][1] = __builtin_amdgcn_raw_buffer_load_b128(rs, hbase1, hso[q], SC1); }
#define GWAIT() { _Pragma("nounroll") for (unsigned spins = 0;; ++spins) { \
    __builtin_amdgcn_s_sleep(1); asm volatile("" ::: "memory"); LDG(); CNT(cnt_poll); \
    if (GOK() || spins >= SPIN_CAP) break; } asm volatile("" ::: "memory"); }
#define HBAD(q) __any(max(umax4(hr[q][0]), umax4(hr[q][1])) == SENTW)
#define WFQ(q) { _Pragma("unroll") for (int c = 0; c < 8; ++c) wf[q][c] = colfrag(wdb[q], c); \
    _Pragma("unroll") for (int c = 0; c < 8; ++c) asm volatile("" : "+v"(wf[q][c])); }
#define PINPTR() asm volatile("" : "+s"(x), "+s"(wg), "+s"(wu), "+s"(wd), "+s"(ws), "+s"(out))

    const int mt = r & 3, nt = xcd * 7 + (r >> 2);
    const unsigned xreg = (unsigned)(size_t)(LAS unsigned char*)smem + w * 32768;
    const unsigned xot = (mt < 2 ? (mt == 0 ? 0x3210u : 0x3210u) : (mt == 2 ? 0x2310u : 0x2310u)), sot = (mt < 2 ? (mt == 0 ? 0x73652140u : 0x73216504u) : (mt == 2 ? 0x76235410u : 0x36271054u));
#define XP(j) ((xot >> (4 * (j))) & 3)
#define PC(s) ((sot >> (4 * (s))) & 7)
    const int r8 = lane >> 3;
    unsigned xv[8];
#pragma unroll
    for (int R = 0; R < 8; ++R)
      xv[R] = ((mt * 64 + 16 * (((R >> 1) + w) & 3) + 8 * (R & 1) + r8) * D_ + (1 ? 64 : 256) * w + (((lane & 7) ^ (((R & 1) << 2) | (r8 >> 1))) * 8)) * 2
              + 4096 - (R & 3) * 1024;
    unsigned xm[4], xs[4], wso[8], xso[8];
#pragma unroll
    for (int j = 0; j < 4; ++j) { xm[j] = xreg + XP(j) * 8192; xs[j] = XP(j) * (1 ? 512 : 128); }
#pragma unroll
    for (int s = 0; s < 8; ++s) {
      wso[s] = ((PC(s) & 3) * (1 ? 8 : 2) + (PC(s) >> 2)) * (32 * F_ * 2);
      xso[s] = (PC(s) & 3) * 8192 + (PC(s) >> 2) * 64;
    }
    unsigned wl = (((1 ? 64 : 256) * w + 8 * g) * F_ + nt * 64 + (i & 7) * 8) * 2 + F_;
    u64 c2r = 4 * F_;
    asm volatile("" : "+v"(xv[0]), "+v"(xv[1]), "+v"(xv[2]), "+v"(xv[3]), "+v"(xv[4]), "+v"(xv[5]), "+v"(xv[6]), "+v"(xv[7]), "+v"(wl), "+s"(c2r));
    asm volatile("" : "+s"(xm[0]), "+s"(xm[1]), "+s"(xm[2]), "+s"(xm[3]), "+s"(xs[0]), "+s"(xs[1]), "+s"(xs[2]), "+s"(xs[3]), "+s"(wso[0]), "+s"(wso[1]), "+s"(wso[2]));
#define XM0(j) asm volatile("s_mov_b32 m0, %0" :: "s"(xm[j]) : "memory")
#define XM1() asm volatile("s_bitset1_b32 m0, 12" ::: "memory")
#define XNOP() asm volatile("s_nop 0" ::: "memory")
#define LDXD(j, R) asm volatile("buffer_load_dwordx4 %0, %1, %2 offen offset:%3 lds" :: "v"(xv[R]), "s"(rx), "s"(xs[j]), "n"(((R) & 3) * 1024) : "memory")
    XM0(0);
    COMMONA()
    PRODA()
    SB();
    PINPTR();
    FSTAMP(2)
    u64 tag = 0;
    if (!0 || bid >= 224) tag = *(const __attribute__((address_space(4))) u64*)(ws + EP_OFF);
    SB();
  if (bid < 224) {
    const u64 xb = (u64)x - 4096;
    const v4u rx = {(unsigned)xb, (unsigned)(xb >> 32) & 0xffffu, (unsigned)(M_ * D_ * 2 + 4096), 0x00020000u};
    const u64 wp = (u64)(i < 8 ? wg : wu) + wl;
    u64 wa[8][4];
    v4u wb[8][8];
#define ADD64(d, c, sh, a) asm("v_lshl_add_u64 %0, %1, " #sh ", %2" : "=v"(d) : "s"(c), "v"(a))
#define WSL(s, p) { if ((p) == 0) { const u64 o_ = wso[s]; ADD64(wa[s][0], o_, 0, wp); } else if ((p) == 1) ADD64(wa[s][1], c2r, 0, wa[s][0]); \
    else if ((p) == 2) ADD64(wa[s][2], c2r, 1, wa[s][0]); else ADD64(wa[s][3], c2r, 1, wa[s][1]); }
#define LDW(s, j) wb[s][j] = *(const GAS v4u*)((const GAS unsigned char*)wa[s][(j) >> 1] + (((j) & 1) ? F_ : -F_))
#define LDWA(s, j) asm volatile("global_load_dwordx4 %0, %1, off offset:%2" : "=v"(wb[s][j]) : "v"(wa[s][(j) >> 1]), "n"(((j) & 1) ? F_ : -F_) : "memory")
    unsigned tch, tpw[2];
#define TCHW(k) tpw[k] = *(const GAS unsigned*)(wp + tpd[k])
#define TOUCH1() { const u64 ta = lane < 32 ? wp - F_ : xb + (lane < 48 ? xv[0] : xv[4]); \
      asm volatile("global_load_dword %0, %1, off" : "=v"(tch) : "v"(ta) : "memory"); SB(); }
    TOUCH1();
    FSTAMP(3)
    LDXD(0, 0);
    LDXD(0, 1);
    LDXD(0, 2);
    LDXD(0, 3); XM1();
    FSTAMP(4)
    XNOP(); LDXD(0, 4);
    LDXD(0, 5);
    LDXD(0, 6);
    LDXD(0, 7); XM0(1);
    FSTAMP(5)
    WSL(0, 0); LDWA(0, 0);
    LDXD(1, 0);
    LDWA(0, 1);
    LDXD(1, 1);
    FSTAMP(6)
    WSL(0, 1); LDWA(0, 2);
    LDXD(1, 2);
    LDWA(0, 3);
    LDXD(1, 3); XM1();
    FSTAMP(7)
    WSL(0, 2); LDWA(0, 4);
    LDXD(1, 4);
    LDWA(0, 5);
    LDXD(1, 5);
    FSTAMP(8)
    WSL(0, 3); LDWA(0, 6);
    LDXD(1, 6);
    LDWA(0, 7);
    LDXD(1, 7); XM0(2);
    FSTAMP(9)
    WSL(1, 0); LDWA(1, 0);
    LDXD(2, 0);
    LDWA(1, 1);
    LDXD(2, 1);
    FSTAMP(10)
    WSL(1, 1); LDWA(1, 2);
    LDXD(2, 2);
    LDWA(1, 3);
    LDXD(2, 3); XM1();
    FSTAMP(11)
    WSL(1, 2); LDWA(1, 4);
    LDXD(2, 4);
    LDWA(1, 5);
    LDXD(2, 5);
    FSTAMP(12)
    WSL(1, 3); LDWA(1, 6);
    LDXD(2, 6);
    LDWA(1, 7);
    SB();
    COMMON()
    v4u xf[2][4];
    unsigned xa[8];
#define XSL(s) xa[s] = xrd ^ xso[s]
#define LDXF(s, t) xf[(s) & 1][t] = *(const v4u*)(smem + xa[s] + (t) * 2048)
    f32x4 acc[3][8], acc0[8];
#pragma unroll
    for (int t = 0; t < 3; ++t)
#pragma unroll
      for (int c = 0; c < 8; ++c) acc[t][c] = f32x4{0.f, 0.f, 0.f, 0.f};
    unsigned rwa, rra[3];
#define RADDR() { rwa = w * 32768 + lane * 16; _Pragma("unroll") for (int t = 0; t < 3; ++t) rra[t] = rdo[t] + lane * 16; \
    asm volatile("" : "+v"(rwa), "+v"(rra[0]), "+v"(rra[1]), "+v"(rra[2])); }
#define RWR(t, c) *(f32x4*)(smem + rwa + ((t) * 8 + (c)) * 1024) = acc[t][c]
#define RRD(t, c) pr[c][t] = *(const f32x4*)(smem + rra[t] + (c) * 1024)
    v4u F7[8];
    unsigned em = 0;
    f32x4 pr[8][3];
    float hl[8], hh[8];
    unsigned ep_[8], eu_[8]; float e0_[8], e1_[8];
#define EP0(c) { const f32x4 fin = ((acc0[c] + pr[c][0]) + pr[c][1]) + pr[c][2]; \
      const auto sw = __builtin_amdgcn_permlane32_swap(cvt_pk(fin[0], fin[1]), cvt_pk(fin[2], fin[3]), false, false); \
      ep_[c] = sw[0]; eu_[c] = sw[1]; }
#define EP1(c) { e0_[c] = __builtin_amdgcn_rcpf(1.0f + __expf(-bf_lo(ep_[c]))); e1_[c] = __builtin_amdgcn_rcpf(1.0f + __expf(-bf_hi(ep_[c]))); }
#define EP2(c) { const unsigned ps = cvt_pk(bf_lo(ep_[c]) * e0_[c], bf_hi(ep_[c]) * e1_[c]); \
      hl[c] = bf_lo(ps) * bf_lo(eu_[c]); hh[c] = bf_hi(ps) * bf_hi(eu_[c]); }
#define HST(q) { v4u v; \
      v[0] = clampw(cvt_pk(hl[4 * q], hl[4 * q + 1])); v[1] = clampw(cvt_pk(hl[4 * q + 2], hl[4 * q + 3])); \
      v[2] = clampw(cvt_pk(hh[4 * q], hh[4 * q + 1])); v[3] = clampw(cvt_pk(hh[4 * q + 2], hh[4 * q + 3])); \
      __builtin_amdgcn_raw_buffer_store_b128(v, rs, hb + q * 1024, 0, SC1); }

    SB();
    STAMP(1)
    XSL(0);
    VMWAIT(31);
    asm volatile("" : "+v"(tch));
    FSTAMP(13)
    LDXF(0, 1); LDXF(0, 2); LDXF(0, 3); LDXF(0, 0);
    SB();
    v4u fcur, fn;
    asm volatile("s_waitcnt vmcnt(28)" : "+v"(wb[0][0]), "+v"(wb[0][1]) :: "memory"); fcur[0] = perm1(wb[0], 0, 0); SB();
    asm volatile("s_waitcnt vmcnt(24)" : "+v"(wb[0][2]), "+v"(wb[0][3]) :: "memory"); fcur[1] = perm1(wb[0], 0, 1); SB();
    asm volatile("s_waitcnt vmcnt(20)" : "+v"(wb[0][4]), "+v"(wb[0][5]) :: "memory"); fcur[2] = perm1(wb[0], 0, 2); SB();
    asm volatile("s_waitcnt vmcnt(16)" : "+v"(wb[0][6]), "+v"(wb[0][7]) :: "memory"); fcur[3] = perm1(wb[0], 0, 3); SB();
    FSTAMP2(14, fcur)
    SB();
    acc[0][0] = mfma16(fcur, xf[0][1], acc[0][0]); SB();
    fn[0] = perm1(wb[0], 1, 0); fn[1] = perm1(wb[0], 1, 1); SB();
    acc[1][0] = mfma16(fcur, xf[0][2], acc[1][0]); SB();
    fn[2] = perm1(wb[0], 1, 2); fn[3] = perm1(wb[0], 1, 3); SB();
    acc[2][0] = mfma16(fcur, xf[0][3], acc[2][0]); SB();
    LDXD(2, 7); XM0(3); SB();
    MFMAV0(acc0[0], fcur, xf[0][0]); SB();
    WSL(2, 0); LDWA(2, 0); SB();
    fcur = fn;
    acc[0][1] = mfma16(fcur, xf[0][1], acc[0][1]); SB();
    fn[0] = perm1(wb[0], 2, 0); fn[1] = perm1(wb[0], 2, 1); SB();
    acc[1][1] = mfma16(fcur, xf[0][2], acc[1][1]); SB();
    fn[2] = perm1(wb[0], 2, 2); fn[3] = perm1(wb[0], 2, 3); SB();
    acc[2][1] = mfma16(fcur, xf[0][3], acc[2][1]); SB();
    LDXD(3, 0); SB();
    MFMAV0(acc0[1], fcur, xf[0][0]); SB();
    LDWA(2, 1); SB();
    fcur = fn;
    acc[0][2] = mfma16(fcur, xf[0][1], acc[0][2]); SB();
    fn[0] = perm1(wb[0], 3, 0); fn[1] = perm1(wb[0], 3, 1); SB();
    acc[1][2] = mfma16(fcur, xf[0][2], acc[1][2]); SB();
    fn[2] = perm1(wb[0], 3, 2); fn[3] = perm1(wb[0], 3, 3); SB();
    acc[2][2] = mfma16(fcur, xf[0][3], acc[2][2]); SB();
    LDXD(3, 1); SB();
    MFMAV0(acc0[2], fcur, xf[0][0]); SB();
    WSL(2, 1); LDWA(2, 2); SB();
    fcur = fn;
    acc[0][3] = mfma16(fcur, xf[0][1], acc[0][3]); SB();
    fn[0] = perm1(wb[0], 4, 0); fn[1] = perm1(wb[0], 4, 1); SB();
    acc[1][3] = mfma16(fcur, xf[0][2], acc[1][3]); SB();
    fn[2] = perm1(wb[0], 4, 2); fn[3] = perm1(wb[0], 4, 3); SB();
    acc[2][3] = mfma16(fcur, xf[0][3], acc[2][3]); SB();
    LDXD(3, 2); SB();
    MFMAV0(acc0[3], fcur, xf[0][0]); SB();
    LDWA(2, 3); SB();
    VMWAIT(23); SB();
    XSL(1); LDXF(1, 1); SB();
    fcur = fn;
    acc[0][4] = mfma16(fcur, xf[0][1], acc[0][4]); SB();
    fn[0] = perm1(wb[0], 5, 0); fn[1] = perm1(wb[0], 5, 1); SB();
    acc[1][4] = mfma16(fcur, xf[0][2], acc[1][4]); SB();
    fn[2] = perm1(wb[0], 5, 2); fn[3] = perm1(wb[0], 5, 3); SB();
    acc[2][4] = mfma16(fcur, xf[0][3], acc[2][4]); SB();
    LDXD(3, 3); XM1(); SB();
    MFMAV0(acc0[4], fcur, xf[0][0]); SB();
    WSL(2, 2); LDWA(2, 4); SB();
    LDXF(1, 2); SB();
    fcur = fn;
    acc[0][5] = mfma16(fcur, xf[0][1], acc[0][5]); SB();
    fn[0] = perm1(wb[0], 6, 0); fn[1] = perm1(wb[0], 6, 1); SB();
    acc[1][5] = mfma16(fcur, xf[0][2], acc[1][5]); SB();
    fn[2] = perm1(wb[0], 6, 2); fn[3] = perm1(wb[0], 6, 3); SB();
    acc[2][5] = mfma16(fcur, xf[0][3], acc[2][5]); SB();
    LDXD(3, 4); SB();
    MFMAV0(acc0[5], fcur, xf[0][0]); SB();
    LDWA(2, 5); SB();
    LDXF(1, 3); SB();
    fcur = fn;
    acc[0][6] = mfma16(fcur, xf[0][1], acc[0][6]); SB();
    fn[0] = perm1(wb[0], 7, 0); fn[1] = perm1(wb[0], 7, 1); SB();
    acc[1][6] = mfma16(fcur, xf[0][2], acc[1][6]); SB();
    fn[2] = perm1(wb[0], 7, 2); fn[3] = perm1(wb[0], 7, 3); SB();
    acc[2][6] = mfma16(fcur, xf[0][3], acc[2][6]); SB();
    LDXD(3, 5); SB();
    MFMAV0(acc0[6], fcur, xf[0][0]); SB();
    WSL(2, 3); LDWA(2, 6); SB();
    LDXF(1, 0); SB();
    fcur = fn;
    acc[0][7] = mfma16(fcur, xf[0][1], acc[0][7]); SB();
    asm volatile("s_waitcnt vmcnt(22)" : "+v"(wb[1][0]), "+v"(wb[1][1]), "+v"(wb[1][2]), "+v"(wb[1][3]) :: "memory"); fn[0] = perm1(wb[1], 0, 0); fn[1] = perm1(wb[1], 0, 1); SB();
    acc[1][7] = mfma16(fcur, xf[0][2], acc[1][7]); SB();
    asm volatile("s_waitcnt vmcnt(14)" : "+v"(wb[1][4]), "+v"(wb[1][5]), "+v"(wb[1][6]), "+v"(wb[1][7]) :: "memory"); fn[2] = perm1(wb[1], 0, 2); fn[3] = perm1(wb[1], 0, 3); SB();
    acc[2][7] = mfma16(fcur, xf[0][3], acc[2][7]); SB();
    LDXD(3, 6); SB();
    MFMAV0(acc0[7], fcur, xf[0][0]); SB();
    LDWA(2, 7); SB();
    fcur = fn;
    STAMP(2)
    acc[0][0] = mfma16(fcur, xf[1][1], acc[0][0]); SB();
    fn[0] = perm1(wb[1], 1, 0); fn[1] = perm1(wb[1], 1, 1); SB();
    acc[1][0] = mfma16(fcur, xf[1][2], acc[1][0]); SB();
    fn[2] = perm1(wb[1], 1, 2); fn[3] = perm1(wb[1], 1, 3); SB();
    acc[2][0] = mfma16(fcur, xf[1][3], acc[2][0]); SB();
    LDXD(3, 7); SB();
    MFMAV(acc0[0], fcur, xf[1][0]); SB();
    WSL(3, 0); LDW(3, 0); SB();
    fcur = fn;
    acc[0][1] = mfma16(fcur, xf[1][1], acc[0][1]); SB();
    fn[0] = perm1(wb[1], 2, 0); fn[1] = perm1(wb[1], 2, 1); SB();
    acc[1][1] = mfma16(fcur, xf[1][2], acc[1][1]); SB();
    fn[2] = perm1(wb[1], 2, 2); fn[3] = perm1(wb[1], 2, 3); SB();
    acc[2][1] = mfma16(fcur, xf[1][3], acc[2][1]); SB();
    LDW(3, 1); SB();
    MFMAV(acc0[1], fcur, xf[1][0]); SB();
    WSL(3, 1); LDW(3, 2); SB();
    fcur = fn;
    acc[0][2] = mfma16(fcur, xf[1][1], acc[0][2]); SB();
    fn[0] = perm1(wb[1], 3, 0); fn[1] = perm1(wb[1], 3, 1); SB();
    acc[1][2] = mfma16(fcur, xf[1][2], acc[1][2]); SB();
    fn[2] = perm1(wb[1], 3, 2); fn[3] = perm1(wb[1], 3, 3); SB();
    acc[2][2] = mfma16(fcur, xf[1][3], acc[2][2]); SB();
    LDW(3, 3); SB();
    MFMAV(acc0[2], fcur, xf[1][0]); SB();
    WSL(3, 2); LDW(3, 4); SB();
    fcur = fn;
    acc[0][3] = mfma16(fcur, xf[1][1], acc[0][3]); SB();
    fn[0] = perm1(wb[1], 4, 0); fn[1] = perm1(wb[1], 4, 1); SB();
    acc[1][3] = mfma16(fcur, xf[1][2], acc[1][3]); SB();
    fn[2] = perm1(wb[1], 4, 2); fn[3] = perm1(wb[1], 4, 3); SB();
    acc[2][3] = mfma16(fcur, xf[1][3], acc[2][3]); SB();
    LDW(3, 5); SB();
    MFMAV(acc0[3], fcur, xf[1][0]); SB();
    WSL(3, 3); LDW(3, 6); SB();
    XSL(2); LDXF(2, 1); SB();
    fcur = fn;
    acc[0][4] = mfma16(fcur, xf[1][1], acc[0][4]); SB();
    fn[0] = perm1(wb[1], 5, 0); fn[1] = perm1(wb[1], 5, 1); SB();
    acc[1][4] = mfma16(fcur, xf[1][2], acc[1][4]); SB();
    fn[2] = perm1(wb[1], 5, 2); fn[3] = perm1(wb[1], 5, 3); SB();
    acc[2][4] = mfma16(fcur, xf[1][3], acc[2][4]); SB();
    LDW(3, 7); SB();
    MFMAV(acc0[4], fcur, xf[1][0]); SB();
    WSL(4, 0); LDW(4, 0); SB();
    LDXF(2, 2); SB();
    fcur = fn;
    acc[0][5] = mfma16(fcur, xf[1][1], acc[0][5]); SB();
    fn[0] = perm1(wb[1], 6, 0); fn[1] = perm1(wb[1], 6, 1); SB();
    acc[1][5] = mfma16(fcur, xf[1][2], acc[1][5]); SB();
    fn[2] = perm1(wb[1], 6, 2); fn[3] = perm1(wb[1], 6, 3); SB();
    acc[2][5] = mfma16(fcur, xf[1][3], acc[2][5]); SB();
    LDW(4, 1); SB();
    MFMAV(acc0[5], fcur, xf[1][0]); SB();
    WSL(4, 1); LDW(4, 2); SB();
    LDXF(2, 3); SB();
    fcur = fn;
    acc[0][6] = mfma16(fcur, xf[1][1], acc[0][6]); SB();
    fn[0] = perm1(wb[1], 7, 0); fn[1] = perm1(wb[1], 7, 1); SB();
    acc[1][6] = mfma16(fcur, xf[1][2], acc[1][6]); SB();
    fn[2] = perm1(wb[1], 7, 2); fn[3] = perm1(wb[1], 7, 3); SB();
    acc[2][6] = mfma16(fcur, xf[1][3], acc[2][6]); SB();
    LDW(4, 3); SB();
    MFMAV(acc0[6], fcur, xf[1][0]); SB();
    WSL(4, 2); LDW(4, 4); SB();
    LDXF(2, 0); SB();
    fcur = fn;
    acc[0][7] = mfma16(fcur, xf[1][1], acc[0][7]); SB();
    asm volatile("s_waitcnt vmcnt(22)" : "+v"(wb[2][0]), "+v"(wb[2][1]), "+v"(wb[2][2]), "+v"(wb[2][3]) :: "memory"); fn[0] = perm1(wb[2], 0, 0); fn[1] = perm1(wb[2], 0, 1); SB();
    acc[1][7] = mfma16(fcur, xf[1][2], acc[1][7]); SB();
    asm volatile("s_waitcnt vmcnt(14)" : "+v"(wb[2][4]), "+v"(wb[2][5]), "+v"(wb[2][6]), "+v"(wb[2][7]) :: "memory"); fn[2] = perm1(wb[2], 0, 2); fn[3] = perm1(wb[2], 0, 3); SB();
    acc[2][7] = mfma16(fcur, xf[1][3], acc[2][7]); SB();
    LDW(4, 5); SB();
    MFMAV(acc0[7], fcur, xf[1][0]); SB();
    WSL(4, 3); LDW(4, 6); SB();
    fcur = fn;
    STAMP(3)
    acc[0][0] = mfma16(fcur, xf[0][1], acc[0][0]); SB();
    fn[0] = perm1(wb[2], 1, 0); fn[1] = perm1(wb[2], 1, 1); SB();
    acc[1][0] = mfma16(fcur, xf[0][2], acc[1][0]); SB();
    fn[2] = perm1(wb[2], 1, 2); fn[3] = perm1(wb[2], 1, 3); SB();
    acc[2][0] = mfma16(fcur, xf[0][3], acc[2][0]); SB();
    LDW(4, 7); SB();
    MFMAV(acc0[0], fcur, xf[0][0]); SB();
    fcur = fn;
    acc[0][1] = mfma16(fcur, xf[0][1], acc[0][1]); SB();
    fn[0] = perm1(wb[2], 2, 0); fn[1] = perm1(wb[2], 2, 1); SB();
    acc[1][1] = mfma16(fcur, xf[0][2], acc[1][1]); SB();
    fn[2] = perm1(wb[2], 2, 2); fn[3] = perm1(wb[2], 2, 3); SB();
    acc[2][1] = mfma16(fcur, xf[0][3], acc[2][1]); SB();
    WSL(5, 0); LDW(5, 0); SB();
    MFMAV(acc0[1], fcur, xf[0][0]); SB();
    fcur = fn;
    acc[0][2] = mfma16(fcur, xf[0][1], acc[0][2]); SB();
    fn[0] = perm1(wb[2], 3, 0); fn[1] = perm1(wb[2], 3, 1); SB();
    acc[1][2] = mfma16(fcur, xf[0][2], acc[1][2]); SB();
    fn[2] = perm1(wb[2], 3, 2); fn[3] = perm1(wb[2], 3, 3); SB();
    acc[2][2] = mfma16(fcur, xf[0][3], acc[2][2]); SB();
    LDW(5, 1); SB();
    MFMAV(acc0[2], fcur, xf[0][0]); SB();
    fcur = fn;
    acc[0][3] = mfma16(fcur, xf[0][1], acc[0][3]); SB();
    fn[0] = perm1(wb[2], 4, 0); fn[1] = perm1(wb[2], 4, 1); SB();
    acc[1][3] = mfma16(fcur, xf[0][2], acc[1][3]); SB();
    fn[2] = perm1(wb[2], 4, 2); fn[3] = perm1(wb[2], 4, 3); SB();
    acc[2][3] = mfma16(fcur, xf[0][3], acc[2][3]); SB();
    WSL(5, 1); LDW(5, 2); SB();
    MFMAV(acc0[3], fcur, xf[0][0]); SB();
    LDW(5, 3); SB();
    VMWAIT(36); SB();
    XSL(3); LDXF(3, 1); SB();
    fcur = fn;
    acc[0][4] = mfma16(fcur, xf[0][1], acc[0][4]); SB();
    fn[0] = perm1(wb[2], 5, 0); fn[1] = perm1(wb[2], 5, 1); SB();
    acc[1][4] = mfma16(fcur, xf[0][2], acc[1][4]); SB();
    fn[2] = perm1(wb[2], 5, 2); fn[3] = perm1(wb[2], 5, 3); SB();
    acc[2][4] = mfma16(fcur, xf[0][3], acc[2][4]); SB();
    WSL(5, 2); LDW(5, 4); SB();
    MFMAV(acc0[4], fcur, xf[0][0]); SB();
    LDXF(3, 2); SB();
    fcur = fn;
    acc[0][5] = mfma16(fcur, xf[0][1], acc[0][5]); SB();
    fn[0] = perm1(wb[2], 6, 0); fn[1] = perm1(wb[2], 6, 1); SB();
    acc[1][5] = mfma16(fcur, xf[0][2], acc[1][5]); SB();
    fn[2] = perm1(wb[2], 6, 2); fn[3] = perm1(wb[2], 6, 3); SB();
    acc[2][5] = mfma16(fcur, xf[0][3], acc[2][5]); SB();
    LDW(5, 5); SB();
    MFMAV(acc0[5], fcur, xf[0][0]); SB();
    LDXF(3, 3); SB();
    fcur = fn;
    acc[0][6] = mfma16(fcur, xf[0][1], acc[0][6]); SB();
    fn[0] = perm1(wb[2], 7, 0); fn[1] = perm1(wb[2], 7, 1); SB();
    acc[1][6] = mfma16(fcur, xf[0][2], acc[1][6]); SB();
    fn[2] = perm1(wb[2], 7, 2); fn[3] = perm1(wb[2], 7, 3); SB();
    acc[2][6] = mfma16(fcur, xf[0][3], acc[2][6]); SB();
    WSL(5, 3); LDW(5, 6); SB();
    MFMAV(acc0[6], fcur, xf[0][0]); SB();
    LDXF(3, 0); SB();
    fcur = fn;
    acc[0][7] = mfma16(fcur, xf[0][1], acc[0][7]); SB();
    fn[0] = perm1(wb[3], 0, 0); fn[1] = perm1(wb[3], 0, 1); SB();
    acc[1][7] = mfma16(fcur, xf[0][2], acc[1][7]); SB();
    fn[2] = perm1(wb[3], 0, 2); fn[3] = perm1(wb[3], 0, 3); SB();
    acc[2][7] = mfma16(fcur, xf[0][3], acc[2][7]); SB();
    LDW(5, 7); SB();
    MFMAV(acc0[7], fcur, xf[0][0]); SB();
    fcur = fn;
    STAMP(4)
    acc[0][0] = mfma16(fcur, xf[1][1], acc[0][0]); SB();
    fn[0] = perm1(wb[3], 1, 0); fn[1] = perm1(wb[3], 1, 1); SB();
    acc[1][0] = mfma16(fcur, xf[1][2], acc[1][0]); SB();
    fn[2] = perm1(wb[3], 1, 2); fn[3] = perm1(wb[3], 1, 3); SB();
    acc[2][0] = mfma16(fcur, xf[1][3], acc[2][0]); SB();
    WSL(6, 0); LDW(6, 0); SB();
    MFMAV(acc0[0], fcur, xf[1][0]); SB();
    fcur = fn;
    acc[0][1] = mfma16(fcur, xf[1][1], acc[0][1]); SB();
    fn[0] = perm1(wb[3], 2, 0); fn[1] = perm1(wb[3], 2, 1); SB();
    acc[1][1] = mfma16(fcur, xf[1][2], acc[1][1]); SB();
    fn[2] = perm1(wb[3], 2, 2); fn[3] = perm1(wb[3], 2, 3); SB();
    acc[2][1] = mfma16(fcur, xf[1][3], acc[2][1]); SB();
    LDW(6, 1); SB();
    MFMAV(acc0[1], fcur, xf[1][0]); SB();
    fcur = fn;
    acc[0][2] = mfma16(fcur, xf[1][1], acc[0][2]); SB();
    fn[0] = perm1(wb[3], 3, 0); fn[1] = perm1(wb[3], 3, 1); SB();
    acc[1][2] = mfma16(fcur, xf[1][2], acc[1][2]); SB();
    fn[2] = perm1(wb[3], 3, 2); fn[3] = perm1(wb[3], 3, 3); SB();
    acc[2][2] = mfma16(fcur, xf[1][3], acc[2][2]); SB();
    WSL(6, 1); LDW(6, 2); SB();
    MFMAV(acc0[2], fcur, xf[1][0]); SB();
    fcur = fn;
    acc[0][3] = mfma16(fcur, xf[1][1], acc[0][3]); SB();
    fn[0] = perm1(wb[3], 4, 0); fn[1] = perm1(wb[3], 4, 1); SB();
    acc[1][3] = mfma16(fcur, xf[1][2], acc[1][3]); SB();
    fn[2] = perm1(wb[3], 4, 2); fn[3] = perm1(wb[3], 4, 3); SB();
    acc[2][3] = mfma16(fcur, xf[1][3], acc[2][3]); SB();
    LDW(6, 3); SB();
    MFMAV(acc0[3], fcur, xf[1][0]); SB();
    XSL(4); LDXF(4, 1); SB();
    fcur = fn;
    acc[0][4] = mfma16(fcur, xf[1][1], acc[0][4]); SB();
    fn[0] = perm1(wb[3], 5, 0); fn[1] = perm1(wb[3], 5, 1); SB();
    acc[1][4] = mfma16(fcur, xf[1][2], acc[1][4]); SB();
    fn[2] = perm1(wb[3], 5, 2); fn[3] = perm1(wb[3], 5, 3); SB();
    acc[2][4] = mfma16(fcur, xf[1][3], acc[2][4]); SB();
    WSL(6, 2); LDW(6, 4); SB();
    MFMAV(acc0[4], fcur, xf[1][0]); SB();
    LDXF(4, 2); SB();
    fcur = fn;
    acc[0][5] = mfma16(fcur, xf[1][1], acc[0][5]); SB();
    fn[0] = perm1(wb[3], 6, 0); fn[1] = perm1(wb[3], 6, 1); SB();
    acc[1][5] = mfma16(fcur, xf[1][2], acc[1][5]); SB();
    fn[2] = perm1(wb[3], 6, 2); fn[3] = perm1(wb[3], 6, 3); SB();
    acc[2][5] = mfma16(fcur, xf[1][3], acc[2][5]); SB();
    LDW(6, 5); SB();
    MFMAV(acc0[5], fcur, xf[1][0]); SB();
    LDXF(4, 3); SB();
    fcur = fn;
    acc[0][6] = mfma16(fcur, xf[1][1], acc[0][6]); SB();
    fn[0] = perm1(wb[3], 7, 0); fn[1] = perm1(wb[3], 7, 1); SB();
    acc[1][6] = mfma16(fcur, xf[1][2], acc[1][6]); SB();
    fn[2] = perm1(wb[3], 7, 2); fn[3] = perm1(wb[3], 7, 3); SB();
    acc[2][6] = mfma16(fcur, xf[1][3], acc[2][6]); SB();
    WSL(6, 3); LDW(6, 6); SB();
    MFMAV(acc0[6], fcur, xf[1][0]); SB();
    LDXF(4, 0); SB();
    fcur = fn;
    acc[0][7] = mfma16(fcur, xf[1][1], acc[0][7]); SB();
    fn[0] = perm1(wb[4], 0, 0); fn[1] = perm1(wb[4], 0, 1); SB();
    acc[1][7] = mfma16(fcur, xf[1][2], acc[1][7]); SB();
    fn[2] = perm1(wb[4], 0, 2); fn[3] = perm1(wb[4], 0, 3); SB();
    acc[2][7] = mfma16(fcur, xf[1][3], acc[2][7]); SB();
    LDW(6, 7); SB();
    MFMAV(acc0[7], fcur, xf[1][0]); SB();
    fcur = fn;
    STAMP(5)
    acc[0][0] = mfma16(fcur, xf[0][1], acc[0][0]); SB();
    fn[0] = perm1(wb[4], 1, 0); fn[1] = perm1(wb[4], 1, 1); SB();
    acc[1][0] = mfma16(fcur, xf[0][2], acc[1][0]); SB();
    fn[2] = perm1(wb[4], 1, 2); fn[3] = perm1(wb[4], 1, 3); SB();
    acc[2][0] = mfma16(fcur, xf[0][3], acc[2][0]); SB();
    WSL(7, 0); LDW(7, 0); SB();
    MFMAV(acc0[0], fcur, xf[0][0]); SB();
    fcur = fn;
    acc[0][1] = mfma16(fcur, xf[0][1], acc[0][1]); SB();
    fn[0] = perm1(wb[4], 2, 0); fn[1] = perm1(wb[4], 2, 1); SB();
    acc[1][1] = mfma16(fcur, xf[0][2], acc[1][1]); SB();
    fn[2] = perm1(wb[4], 2, 2); fn[3] = perm1(wb[4], 2, 3); SB();
    acc[2][1] = mfma16(fcur, xf[0][3], acc[2][1]); SB();
    LDW(7, 1); SB();
    MFMAV(acc0[1], fcur, xf[0][0]); SB();
    fcur = fn;
    acc[0][2] = mfma16(fcur, xf[0][1], acc[0][2]); SB();
    fn[0] = perm1(wb[4], 3, 0); fn[1] = perm1(wb[4], 3, 1); SB();
    acc[1][2] = mfma16(fcur, xf[0][2], acc[1][2]); SB();
    fn[2] = perm1(wb[4], 3, 2); fn[3] = perm1(wb[4], 3, 3); SB();
    acc[2][2] = mfma16(fcur, xf[0][3], acc[2][2]); SB();
    WSL(7, 1); LDW(7, 2); SB();
    MFMAV(acc0[2], fcur, xf[0][0]); SB();
    fcur = fn;
    acc[0][3] = mfma16(fcur, xf[0][1], acc[0][3]); SB();
    fn[0] = perm1(wb[4], 4, 0); fn[1] = perm1(wb[4], 4, 1); SB();
    acc[1][3] = mfma16(fcur, xf[0][2], acc[1][3]); SB();
    fn[2] = perm1(wb[4], 4, 2); fn[3] = perm1(wb[4], 4, 3); SB();
    acc[2][3] = mfma16(fcur, xf[0][3], acc[2][3]); SB();
    LDW(7, 3); SB();
    MFMAV(acc0[3], fcur, xf[0][0]); SB();
    VMWAIT(36); SB();
    XSL(5); LDXF(5, 1); SB();
    fcur = fn;
    acc[0][4] = mfma16(fcur, xf[0][1], acc[0][4]); SB();
    fn[0] = perm1(wb[4], 5, 0); fn[1] = perm1(wb[4], 5, 1); SB();
    acc[1][4] = mfma16(fcur, xf[0][2], acc[1][4]); SB();
    fn[2] = perm1(wb[4], 5, 2); fn[3] = perm1(wb[4], 5, 3); SB();
    acc[2][4] = mfma16(fcur, xf[0][3], acc[2][4]); SB();
    WSL(7, 2); LDW(7, 4); SB();
    MFMAV(acc0[4], fcur, xf[0][0]); SB();
     PINTAG(); FLAG(); SB();
    LDXF(5, 2); SB();
    fcur = fn;
    acc[0][5] = mfma16(fcur, xf[0][1], acc[0][5]); SB();
    fn[0] = perm1(wb[4], 6, 0); fn[1] = perm1(wb[4], 6, 1); SB();
    acc[1][5] = mfma16(fcur, xf[0][2], acc[1][5]); SB();
    fn[2] = perm1(wb[4], 6, 2); fn[3] = perm1(wb[4], 6, 3); SB();
    acc[2][5] = mfma16(fcur, xf[0][3], acc[2][5]); SB();
    LDW(7, 5); SB();
    MFMAV(acc0[5], fcur, xf[0][0]); SB();
    LDXF(5, 3); SB();
    fcur = fn;
    acc[0][6] = mfma16(fcur, xf[0][1], acc[0][6]); SB();
    fn[0] = perm1(wb[4], 7, 0); fn[1] = perm1(wb[4], 7, 1); SB();
    acc[1][6] = mfma16(fcur, xf[0][2], acc[1][6]); SB();
    fn[2] = perm1(wb[4], 7, 2); fn[3] = perm1(wb[4], 7, 3); SB();
    acc[2][6] = mfma16(fcur, xf[0][3], acc[2][6]); SB();
    WSL(7, 3); LDW(7, 6); SB();
    MFMAV(acc0[6], fcur, xf[0][0]); SB();
    LDXF(5, 0); SB();
    fcur = fn;
    acc[0][7] = mfma16(fcur, xf[0][1], acc[0][7]); SB();
    fn[0] = perm1(wb[5], 0, 0); fn[1] = perm1(wb[5], 0, 1); SB();
    acc[1][7] = mfma16(fcur, xf[0][2], acc[1][7]); SB();
    fn[2] = perm1(wb[5], 0, 2); fn[3] = perm1(wb[5], 0, 3); SB();
    acc[2][7] = mfma16(fcur, xf[0][3], acc[2][7]); SB();
    LDW(7, 7); SB();
    MFMAV(acc0[7], fcur, xf[0][0]); SB();
    fcur = fn;
    STAMP(6)
    acc[0][0] = mfma16(fcur, xf[1][1], acc[0][0]); SB();
    fn[0] = perm1(wb[5], 1, 0); fn[1] = perm1(wb[5], 1, 1); SB();
    acc[1][0] = mfma16(fcur, xf[1][2], acc[1][0]); SB();
    fn[2] = perm1(wb[5], 1, 2); fn[3] = perm1(wb[5], 1, 3); SB();
    acc[2][0] = mfma16(fcur, xf[1][3], acc[2][0]); SB();
    WD4(); LDW2(0); SB();
    MFMAV(acc0[0], fcur, xf[1][0]); SB();
    fcur = fn;
    acc[0][1] = mfma16(fcur, xf[1][1], acc[0][1]); SB();
    fn[0] = perm1(wb[5], 2, 0); fn[1] = perm1(wb[5], 2, 1); SB();
    acc[1][1] = mfma16(fcur, xf[1][2], acc[1][1]); SB();
    fn[2] = perm1(wb[5], 2, 2); fn[3] = perm1(wb[5], 2, 3); SB();
    acc[2][1] = mfma16(fcur, xf[1][3], acc[2][1]); SB();
    LDW2(1); SB();
    MFMAV(acc0[1], fcur, xf[1][0]); SB();
    fcur = fn;
    acc[0][2] = mfma16(fcur, xf[1][1], acc[0][2]); SB();
    fn[0] = perm1(wb[5], 3, 0); fn[1] = perm1(wb[5], 3, 1); SB();
    acc[1][2] = mfma16(fcur, xf[1][2], acc[1][2]); SB();
    fn[2] = perm1(wb[5], 3, 2); fn[3] = perm1(wb[5], 3, 3); SB();
    acc[2][2] = mfma16(fcur, xf[1][3], acc[2][2]); SB();
    LDW2(2); SB();
    MFMAV(acc0[2], fcur, xf[1][0]); SB();
    fcur = fn;
    acc[0][3] = mfma16(fcur, xf[1][1], acc[0][3]); SB();
    fn[0] = perm1(wb[5], 4, 0); fn[1] = perm1(wb[5], 4, 1); SB();
    acc[1][3] = mfma16(fcur, xf[1][2], acc[1][3]); SB();
    fn[2] = perm1(wb[5], 4, 2); fn[3] = perm1(wb[5], 4, 3); SB();
    acc[2][3] = mfma16(fcur, xf[1][3], acc[2][3]); SB();
    LDW2(3); SB();
    MFMAV(acc0[3], fcur, xf[1][0]); SB();
    XSL(6); LDXF(6, 1); SB();
    fcur = fn;
    acc[0][4] = mfma16(fcur, xf[1][1], acc[0][4]); SB();
    fn[0] = perm1(wb[5], 5, 0); fn[1] = perm1(wb[5], 5, 1); SB();
    acc[1][4] = mfma16(fcur, xf[1][2], acc[1][4]); SB();
    fn[2] = perm1(wb[5], 5, 2); fn[3] = perm1(wb[5], 5, 3); SB();
    acc[2][4] = mfma16(fcur, xf[1][3], acc[2][4]); SB();
    LDW2(4); SB();
    MFMAV(acc0[4], fcur, xf[1][0]); SB();
    LDXF(6, 2); SB();
    fcur = fn;
    acc[0][5] = mfma16(fcur, xf[1][1], acc[0][5]); SB();
    fn[0] = perm1(wb[5], 6, 0); fn[1] = perm1(wb[5], 6, 1); SB();
    acc[1][5] = mfma16(fcur, xf[1][2], acc[1][5]); SB();
    fn[2] = perm1(wb[5], 6, 2); fn[3] = perm1(wb[5], 6, 3); SB();
    acc[2][5] = mfma16(fcur, xf[1][3], acc[2][5]); SB();
    LDW2(5); SB();
    MFMAV(acc0[5], fcur, xf[1][0]); SB();
    LDXF(6, 3); SB();
    fcur = fn;
    acc[0][6] = mfma16(fcur, xf[1][1], acc[0][6]); SB();
    fn[0] = perm1(wb[5], 7, 0); fn[1] = perm1(wb[5], 7, 1); SB();
    acc[1][6] = mfma16(fcur, xf[1][2], acc[1][6]); SB();
    fn[2] = perm1(wb[5], 7, 2); fn[3] = perm1(wb[5], 7, 3); SB();
    acc[2][6] = mfma16(fcur, xf[1][3], acc[2][6]); SB();
    LDW2(6); SB();
    MFMAV(acc0[6], fcur, xf[1][0]); SB();
    LDXF(6, 0); SB();
    fcur = fn;
    acc[0][7] = mfma16(fcur, xf[1][1], acc[0][7]); SB();
    fn[0] = perm1(wb[6], 0, 0); fn[1] = perm1(wb[6], 0, 1); SB();
    acc[1][7] = mfma16(fcur, xf[1][2], acc[1][7]); SB();
    fn[2] = perm1(wb[6], 0, 2); fn[3] = perm1(wb[6], 0, 3); SB();
    acc[2][7] = mfma16(fcur, xf[1][3], acc[2][7]); SB();
    LDW2(7); SB();
    MFMAV(acc0[7], fcur, xf[1][0]); SB();
    fcur = fn;
    STAMP(7)
    acc[0][0] = mfma16(fcur, xf[0][1], acc[0][0]); SB();
    fn[0] = perm1(wb[6], 1, 0); fn[1] = perm1(wb[6], 1, 1); SB();
    acc[1][0] = mfma16(fcur, xf[0][2], acc[1][0]); SB();
    fn[2] = perm1(wb[6], 1, 2); fn[3] = perm1(wb[6], 1, 3); SB();
    acc[2][0] = mfma16(fcur, xf[0][3], acc[2][0]); SB();
    LDW2(8); SB();
    MFMAV(acc0[0], fcur, xf[0][0]); SB();
    fcur = fn;
    acc[0][1] = mfma16(fcur, xf[0][1], acc[0][1]); SB();
    fn[0] = perm1(wb[6], 2, 0); fn[1] = perm1(wb[6], 2, 1); SB();
    acc[1][1] = mfma16(fcur, xf[0][2], acc[1][1]); SB();
    fn[2] = perm1(wb[6], 2, 2); fn[3] = perm1(wb[6], 2, 3); SB();
    acc[2][1] = mfma16(fcur, xf[0][3], acc[2][1]); SB();
    LDW2(9); SB();
    MFMAV(acc0[1], fcur, xf[0][0]); SB();
    fcur = fn;
    acc[0][2] = mfma16(fcur, xf[0][1], acc[0][2]); SB();
    fn[0] = perm1(wb[6], 3, 0); fn[1] = perm1(wb[6], 3, 1); SB();
    acc[1][2] = mfma16(fcur, xf[0][2], acc[1][2]); SB();
    fn[2] = perm1(wb[6], 3, 2); fn[3] = perm1(wb[6], 3, 3); SB();
    acc[2][2] = mfma16(fcur, xf[0][3], acc[2][2]); SB();
    LDW2(10); SB();
    MFMAV(acc0[2], fcur, xf[0][0]); SB();
    fcur = fn;
    acc[0][3] = mfma16(fcur, xf[0][1], acc[0][3]); SB();
    fn[0] = perm1(wb[6], 4, 0); fn[1] = perm1(wb[6], 4, 1); SB();
    acc[1][3] = mfma16(fcur, xf[0][2], acc[1][3]); SB();
    fn[2] = perm1(wb[6], 4, 2); fn[3] = perm1(wb[6], 4, 3); SB();
    acc[2][3] = mfma16(fcur, xf[0][3], acc[2][3]); SB();
    LDW2(11); SB();
    MFMAV(acc0[3], fcur, xf[0][0]); SB();
    XSL(7); LDXF(7, 1); SB();
    fcur = fn;
    acc[0][4] = mfma16(fcur, xf[0][1], acc[0][4]); SB();
    fn[0] = perm1(wb[6], 5, 0); fn[1] = perm1(wb[6], 5, 1); SB();
    acc[1][4] = mfma16(fcur, xf[0][2], acc[1][4]); SB();
    fn[2] = perm1(wb[6], 5, 2); fn[3] = perm1(wb[6], 5, 3); SB();
    acc[2][4] = mfma16(fcur, xf[0][3], acc[2][4]); SB();
    LDW2(12); SB();
    MFMAV(acc0[4], fcur, xf[0][0]); SB();
    LDXF(7, 2); SB();
    fcur = fn;
    acc[0][5] = mfma16(fcur, xf[0][1], acc[0][5]); SB();
    fn[0] = perm1(wb[6], 6, 0); fn[1] = perm1(wb[6], 6, 1); SB();
    acc[1][5] = mfma16(fcur, xf[0][2], acc[1][5]); SB();
    fn[2] = perm1(wb[6], 6, 2); fn[3] = perm1(wb[6], 6, 3); SB();
    acc[2][5] = mfma16(fcur, xf[0][3], acc[2][5]); SB();
    LDW2(13); SB();
    MFMAV(acc0[5], fcur, xf[0][0]); SB();
    LDXF(7, 3); SB();
    fcur = fn;
    acc[0][6] = mfma16(fcur, xf[0][1], acc[0][6]); SB();
    fn[0] = perm1(wb[6], 7, 0); fn[1] = perm1(wb[6], 7, 1); SB();
    acc[1][6] = mfma16(fcur, xf[0][2], acc[1][6]); SB();
    fn[2] = perm1(wb[6], 7, 2); fn[3] = perm1(wb[6], 7, 3); SB();
    acc[2][6] = mfma16(fcur, xf[0][3], acc[2][6]); SB();
    LDW2(14); SB();
    MFMAV(acc0[6], fcur, xf[0][0]); SB();
    LDXF(7, 0); SB();
    fcur = fn;
    acc[0][7] = mfma16(fcur, xf[0][1], acc[0][7]); SB();
    fn[0] = perm1(wb[7], 0, 0); fn[1] = perm1(wb[7], 0, 1); SB();
    acc[1][7] = mfma16(fcur, xf[0][2], acc[1][7]); SB();
    fn[2] = perm1(wb[7], 0, 2); fn[3] = perm1(wb[7], 0, 3); SB();
    acc[2][7] = mfma16(fcur, xf[0][3], acc[2][7]); SB();
    LDW2(15); SB();
    F7[1] = colfrag(wb[7], 1); SB();
    MFMAV(acc0[7], fcur, xf[0][0]); SB();
    fcur = fn;
    STAMP(8)
    F7[0] = fcur;
    RADDR(); SB();
    acc[0][0] = mfma16(F7[0], xf[1][1], acc[0][0]); SB();
    F7[2][0] = perm1(wb[7], 2, 0); SB();
    F7[2][1] = perm1(wb[7], 2, 1); SB();
    acc[1][0] = mfma16(F7[0], xf[1][2], acc[1][0]); SB();
    F7[2][2] = perm1(wb[7], 2, 2); SB();
    F7[2][3] = perm1(wb[7], 2, 3); SB();
    acc[2][0] = mfma16(F7[0], xf[1][3], acc[2][0]); SB();
    F7[3][0] = perm1(wb[7], 3, 0); SB();
    F7[3][1] = perm1(wb[7], 3, 1); SB();
    LDW2(16); SB();
    acc[0][1] = mfma16(F7[1], xf[1][1], acc[0][1]); SB();
    RWR(0, 0); SB();
    F7[3][2] = perm1(wb[7], 3, 2); SB();
    acc[1][1] = mfma16(F7[1], xf[1][2], acc[1][1]); SB();
    RWR(1, 0); SB();
    F7[3][3] = perm1(wb[7], 3, 3); SB();
    acc[2][1] = mfma16(F7[1], xf[1][3], acc[2][1]); SB();
    RWR(2, 0); SB();
    F7[4][0] = perm1(wb[7], 4, 0); SB();
    LDW2(17); SB();
    acc[0][2] = mfma16(F7[2], xf[1][1], acc[0][2]); SB();
    RWR(0, 1); SB();
    F7[4][1] = perm1(wb[7], 4, 1); SB();
    acc[1][2] = mfma16(F7[2], xf[1][2], acc[1][2]); SB();
    F7[4][2] = perm1(wb[7], 4, 2); SB();
    F7[4][3] = perm1(wb[7], 4, 3); SB();
    acc[2][2] = mfma16(F7[2], xf[1][3], acc[2][2]); SB();
    RWR(1, 1); SB();
    F7[5][0] = perm1(wb[7], 5, 0); SB();
    LDW2(18); SB();
    acc[0][3] = mfma16(F7[3], xf[1][1], acc[0][3]); SB();
    F7[5][1] = perm1(wb[7], 5, 1); SB();
    F7[5][2] = perm1(wb[7], 5, 2); SB();
    acc[1][3] = mfma16(F7[3], xf[1][2], acc[1][3]); SB();
    RWR(2, 1); SB();
    F7[5][3] = perm1(wb[7], 5, 3); SB();
    acc[2][3] = mfma16(F7[3], xf[1][3], acc[2][3]); SB();
    F7[6][0] = perm1(wb[7], 6, 0); SB();
    F7[6][1] = perm1(wb[7], 6, 1); SB();
    LDW2(19); SB();
    MFMAV(acc0[0], F7[0], xf[1][0]); SB();
    RWR(0, 2); SB();
    F7[6][2] = perm1(wb[7], 6, 2); SB();
    MFMAV(acc0[1], F7[1], xf[1][0]); SB();
    F7[6][3] = perm1(wb[7], 6, 3); SB();
    F7[7][0] = perm1(wb[7], 7, 0); SB();
    MFMAV(acc0[2], F7[2], xf[1][0]); SB();
    RWR(1, 2); SB();
    F7[7][1] = perm1(wb[7], 7, 1); SB();
    LDW2(20); SB();
    MFMAV(acc0[3], F7[3], xf[1][0]); SB();
    F7[7][2] = perm1(wb[7], 7, 2); SB();
    F7[7][3] = perm1(wb[7], 7, 3); SB();
    acc[0][4] = mfma16(F7[4], xf[1][1], acc[0][4]); SB();
    RWR(2, 2); SB();
    acc[1][4] = mfma16(F7[4], xf[1][2], acc[1][4]); SB();
    LDW2(21); SB();
    acc[2][4] = mfma16(F7[4], xf[1][3], acc[2][4]); SB();
    RWR(0, 3); SB();
    acc[0][5] = mfma16(F7[5], xf[1][1], acc[0][5]); SB();
    acc[1][5] = mfma16(F7[5], xf[1][2], acc[1][5]); SB();
    RWR(1, 3); SB();
    LDW2(22); SB();
    acc[2][5] = mfma16(F7[5], xf[1][3], acc[2][5]); SB();
    acc[0][6] = mfma16(F7[6], xf[1][1], acc[0][6]); SB();
    RWR(2, 3); SB();
    acc[1][6] = mfma16(F7[6], xf[1][2], acc[1][6]); SB();
    LDW2(23); SB();
    acc[2][6] = mfma16(F7[6], xf[1][3], acc[2][6]); SB();
    acc[0][7] = mfma16(F7[7], xf[1][1], acc[0][7]); SB();
    acc[1][7] = mfma16(F7[7], xf[1][2], acc[1][7]); SB();
    LDW2(24); SB();
    acc[2][7] = mfma16(F7[7], xf[1][3], acc[2][7]); SB();
    MFMAV(acc0[4], F7[4], xf[1][0]); SB();
    MFMAV(acc0[5], F7[5], xf[1][0]); SB();
    LDW2(25); SB();
    MFMAV(acc0[6], F7[6], xf[1][0]); SB();
    MFMAV(acc0[7], F7[7], xf[1][0]); SB();
    BARN(0); SB();
    STAMP(9)
    RRD(0, 0); SB();
    RRD(1, 0); SB();
    RRD(2, 0); SB();
    RRD(0, 1); SB();
    RRD(1, 1); SB();
    RRD(2, 1); SB();
    LDW2(26); SB();
    RRD(0, 2); SB();
    RRD(1, 2); SB();
    RRD(2, 2); SB();
    RRD(0, 3); SB();
    RRD(1, 3); SB();
    RRD(2, 3); SB();
    LDW2(27); SB();
    LDG(); SB();
    RWR(0, 4); SB();
    RWR(1, 4); SB();
    LDW2(28); LDW2(29); SB();
    EP0(0); EP0(1); SB();
    RWR(2, 4); SB();
    RWR(0, 5); SB();
    LDW2(30); LDW2(31); SB();
    EP1(0); EP1(1); SB();
    RWR(1, 5); SB();
    RWR(2, 5); SB();
    LDW2(32); LDW2(33); SB();
    EP2(0); EP2(1); SB();
    RWR(0, 6); SB();
    RWR(1, 6); SB();
    LDW2(34); LDW2(35); SB();
    EP0(2); EP0(3); SB();
    RWR(2, 6); SB();
    RWR(0, 7); SB();
    LDW2(36); LDW2(37); SB();
    EP1(2); EP1(3); SB();
    RWR(1, 7); SB();
    RWR(2, 7); SB();
    LDW2(38); LDW2(39); SB();
    EP2(2); EP2(3); SB();
    if (__builtin_expect(!GOK(), 0)) GWAIT()
    HST(0); SB();
    STAMP(8)
    BARN(0); SB();
    RRD(0, 4); SB();
    RRD(1, 4); SB();
    RRD(2, 4); SB();
    RRD(0, 5); SB();
    RRD(1, 5); SB();
    RRD(2, 5); SB();
    LDW2(40); SB();
    RRD(0, 6); SB();
    RRD(1, 6); SB();
    RRD(2, 6); SB();
    RRD(0, 7); SB();
    RRD(1, 7); SB();
    RRD(2, 7); SB();
    LDW2(41); SB();
    STAMP(10)
    LDW2(42); LDW2(43); SB();
    EP0(4); EP0(5); SB();
    LDW2(44); LDW2(45); SB();
    EP1(4); EP1(5); SB();
    LDW2(46); LDW2(47); SB();
    EP2(4); EP2(5); SB();
    LDW2(48); LDW2(49); SB();
    EP0(6); EP0(7); SB();
    LDW2(50); LDW2(51); SB();
    EP1(6); EP1(7); SB();
    LDW2(52); LDW2(53); SB();
    EP2(6); EP2(7); SB();
    HST(1); SB();
    STAMP(11)
    {
      v4u sl[3], wf[7][8];
      LDW2(54);
      LDW2(55);
      int hbase1 = hbase + KS2 * 1024;
      asm volatile("" : "+v"(hbase1));
      int xfo = lane == 0 ? (int)XF_OFF + bid * 128 + w * 8 : 0x7ffffff0;
      int xfi = (int)XF_OFF + (r * 8 + 2 * (s2 ^ (((lane >> 2) & 3) % 3 + 1)) + (xcd & 1)) * 128 + (lane & 3) * 8;
      asm volatile("" : "+v"(xfo), "+v"(xfi));
      v2u xg;
      unsigned t32 = ((unsigned)tag + (unsigned)(tag >> 32) * 0x85EBCA6Bu) * 0x9E3779B1u; asm volatile("" : "+s"(t32));
      SB();
      WFQ(0)
      WFQ(1)
      WFQ(2)
      WFQ(3)
      SB();
      LDH(0) SB();
      LDH(1) SB();
      LDH(2) SB();
      SB();
      WFQ(4)
      WFQ(5)
      WFQ(6)
      SB();
      LDH(3) SB();
      LDH(4) SB();
      LDH(5) SB();
      LDH(6) SB();
      SB();
      STAMP(12)
      unsigned r2a, r2b, r2r[4];
      f32x4 acc2[2][8];
      HPIN(0) bool nb = HBAD(0);
      if (__builtin_expect(nb, 0)) {
        _Pragma("nounroll") for (unsigned spins = 0; spins < SPIN_CAP; ++spins) {
          CNT(cnt_h); __builtin_amdgcn_s_sleep(2); asm volatile("" ::: "memory");
          LDH(0)
          if (!HBAD(0)) break;
        }
        LDH(1) SB();
        LDH(2) SB();
        LDH(3) SB();
        LDH(4) SB();
        LDH(5) SB();
        LDH(6) SB();
      }
      STAMP(13)
      SB();
      acc2[0][0] = mfma16(hr[0][0], wf[0][0], f32x4{0.f, 0.f, 0.f, 0.f}); SB();
      acc2[1][0] = mfma16(hr[0][1], wf[0][0], f32x4{0.f, 0.f, 0.f, 0.f}); SB();
      acc2[0][1] = mfma16(hr[0][0], wf[0][1], f32x4{0.f, 0.f, 0.f, 0.f}); SB();
      acc2[1][1] = mfma16(hr[0][1], wf[0][1], f32x4{0.f, 0.f, 0.f, 0.f}); SB();
      acc2[0][2] = mfma16(hr[0][0], wf[0][2], f32x4{0.f, 0.f, 0.f, 0.f}); SB();
      acc2[1][2] = mfma16(hr[0][1], wf[0][2], f32x4{0.f, 0.f, 0.f, 0.f}); SB();
      acc2[0][3] = mfma16(hr[0][0], wf[0][3], f32x4{0.f, 0.f, 0.f, 0.f}); SB();
      acc2[1][3] = mfma16(hr[0][1], wf[0][3], f32x4{0.f, 0.f, 0.f, 0.f}); SB();
      acc2[0][4] = mfma16(hr[0][0], wf[0][4], f32x4{0.f, 0.f, 0.f, 0.f}); SB();
      acc2[1][4] = mfma16(hr[0][1], wf[0][4], f32x4{0.f, 0.f, 0.f, 0.f}); SB();
      acc2[0][5] = mfma16(hr[0][0], wf[0][5], f32x4{0.f, 0.f, 0.f, 0.f}); SB();
      acc2[1][5] = mfma16(hr[0][1], wf[0][5], f32x4{0.f, 0.f, 0.f, 0.f}); SB();
      HPIN(1) nb = HBAD(1); SB();
      acc2[0][6] = mfma16(hr[0][0], wf[0][6], f32x4{0.f, 0.f, 0.f, 0.f}); SB();
      acc2[1][6] = mfma16(hr[0][1], wf[0][6], f32x4{0.f, 0.f, 0.f, 0.f}); SB();
      acc2[0][7] = mfma16(hr[0][0], wf[0][7], f32x4{0.f, 0.f, 0.f, 0.f}); SB();
      acc2[1][7] = mfma16(hr[0][1], wf[0][7], f32x4{0.f, 0.f, 0.f, 0.f}); SB();
      if (__builtin_expect(nb, 0)) {
        _Pragma("nounroll") for (unsigned spins = 0; spins < SPIN_CAP; ++spins) {
          CNT(cnt_h); __builtin_amdgcn_s_sleep(2); asm volatile("" ::: "memory");
          LDH(1)
          if (!HBAD(1)) break;
        }
        LDH(2) SB();
        LDH(3) SB();
        LDH(4) SB();
        LDH(5) SB();
        LDH(6) SB();
      }
      SB();
      acc2[0][0] = mfma16(hr[1][0], wf[1][0], acc2[0][0]); SB();
      acc2[1][0] = mfma16(hr[1][1], wf[1][0], acc2[1][0]); SB();
      acc2[0][1] = mfma16(hr[1][0], wf[1][1], acc2[0][1]); SB();
      acc2[1][1] = mfma16(hr[1][1], wf[1][1], acc2[1][1]); SB();
      acc2[0][2] = mfma16(hr[1][0], wf[1][2], acc2[0][2]); SB();
      acc2[1][2] = mfma16(hr[1][1], wf[1][2], acc2[1][2]); SB();
      acc2[0][3] = mfma16(hr[1][0], wf[1][3], acc2[0][3]); SB();
      acc2[1][3] = mfma16(hr[1][1], wf[1][3], acc2[1][3]); SB();
      acc2[0][4] = mfma16(hr[1][0], wf[1][4], acc2[0][4]); SB();
      acc2[1][4] = mfma16(hr[1][1], wf[1][4], acc2[1][4]); SB();
      acc2[0][5] = mfma16(hr[1][0], wf[1][5], acc2[0][5]); SB();
      acc2[1][5] = mfma16(hr[1][1], wf[1][5], acc2[1][5]); SB();
      HPIN(2) nb = HBAD(2); SB();
      acc2[0][6] = mfma16(hr[1][0], wf[1][6], acc2[0][6]); SB();
      acc2[1][6] = mfma16(hr[1][1], wf[1][6], acc2[1][6]); SB();
      acc2[0][7] = mfma16(hr[1][0], wf[1][7], acc2[0][7]); SB();
      acc2[1][7] = mfma16(hr[1][1], wf[1][7], acc2[1][7]); SB();
      if (__builtin_expect(nb, 0)) {
        _Pragma("nounroll") for (unsigned spins = 0; spins < SPIN_CAP; ++spins) {
          CNT(cnt_h); __builtin_amdgcn_s_sleep(2); asm volatile("" ::: "memory");
          LDH(2)
          if (!HBAD(2)) break;
        }
        LDH(3) SB();
        LDH(4) SB();
        LDH(5) SB();
        LDH(6) SB();
      }
      SB();
      acc2[0][0] = mfma16(hr[2][0], wf[2][0], acc2[0][0]); SB();
      acc2[1][0] = mfma16(hr[2][1], wf[2][0], acc2[1][0]); SB();
      acc2[0][1] = mfma16(hr[2][0], wf[2][1], acc2[0][1]); SB();
      acc2[1][1] = mfma16(hr[2][1], wf[2][1], acc2[1][1]); SB();
      acc2[0][2] = mfma16(hr[2][0], wf[2][2], acc2[0][2]); SB();
      acc2[1][2] = mfma16(hr[2][1], wf[2][2], acc2[1][2]); SB();
      acc2[0][3] = mfma16(hr[2][0], wf[2][3], acc2[0][3]); SB();
      acc2[1][3] = mfma16(hr[2][1], wf[2][3], acc2[1][3]); SB();
      acc2[0][4] = mfma16(hr[2][0], wf[2][4], acc2[0][4]); SB();
      acc2[1][4] = mfma16(hr[2][1], wf[2][4], acc2[1][4]); SB();
      acc2[0][5] = mfma16(hr[2][0], wf[2][5], acc2[0][5]); SB();
      acc2[1][5] = mfma16(hr[2][1], wf[2][5], acc2[1][5]); SB();
      HPIN(3) nb = HBAD(3); SB();
      acc2[0][6] = mfma16(hr[2][0], wf[2][6], acc2[0][6]); SB();
      acc2[1][6] = mfma16(hr[2][1], wf[2][6], acc2[1][6]); SB();
      acc2[0][7] = mfma16(hr[2][0], wf[2][7], acc2[0][7]); SB();
      acc2[1][7] = mfma16(hr[2][1], wf[2][7], acc2[1][7]); SB();
      if (__builtin_expect(nb, 0)) {
        _Pragma("nounroll") for (unsigned spins = 0; spins < SPIN_CAP; ++spins) {
          CNT(cnt_h); __builtin_amdgcn_s_sleep(2); asm volatile("" ::: "memory");
          LDH(3)
          if (!HBAD(3)) break;
        }
        LDH(4) SB();
        LDH(5) SB();
        LDH(6) SB();
      }
      SB();
      acc2[0][0] = mfma16(hr[3][0], wf[3][0], acc2[0][0]); SB();
      acc2[1][0] = mfma16(hr[3][1], wf[3][0], acc2[1][0]); SB();
      acc2[0][1] = mfma16(hr[3][0], wf[3][1], acc2[0][1]); SB();
      acc2[1][1] = mfma16(hr[3][1], wf[3][1], acc2[1][1]); SB();
      acc2[0][2] = mfma16(hr[3][0], wf[3][2], acc2[0][2]); SB();
      acc2[1][2] = mfma16(hr[3][1], wf[3][2], acc2[1][2]); SB();
      acc2[0][3] = mfma16(hr[3][0], wf[3][3], acc2[0][3]); SB();
      acc2[1][3] = mfma16(hr[3][1], wf[3][3], acc2[1][3]); SB();
      acc2[0][4] = mfma16(hr[3][0], wf[3][4], acc2[0][4]); SB();
      acc2[1][4] = mfma16(hr[3][1], wf[3][4], acc2[1][4]); SB();
      acc2[0][5] = mfma16(hr[3][0], wf[3][5], acc2[0][5]); SB();
      acc2[1][5] = mfma16(hr[3][1], wf[3][5], acc2[1][5]); SB();
      HPIN(4) nb = HBAD(4); SB();
      acc2[0][6] = mfma16(hr[3][0], wf[3][6], acc2[0][6]); SB();
      acc2[1][6] = mfma16(hr[3][1], wf[3][6], acc2[1][6]); SB();
      acc2[0][7] = mfma16(hr[3][0], wf[3][7], acc2[0][7]); SB();
      acc2[1][7] = mfma16(hr[3][1], wf[3][7], acc2[1][7]); SB();
      if (__builtin_expect(nb, 0)) {
        _Pragma("nounroll") for (unsigned spins = 0; spins < SPIN_CAP; ++spins) {
          CNT(cnt_h); __builtin_amdgcn_s_sleep(2); asm volatile("" ::: "memory");
          LDH(4)
          if (!HBAD(4)) break;
        }
        LDH(5) SB();
        LDH(6) SB();
      }
      SB();
      acc2[0][0] = mfma16(hr[4][0], wf[4][0], acc2[0][0]); SB();
      acc2[1][0] = mfma16(hr[4][1], wf[4][0], acc2[1][0]); SB();
      acc2[0][1] = mfma16(hr[4][0], wf[4][1], acc2[0][1]); SB();
      acc2[1][1] = mfma16(hr[4][1], wf[4][1], acc2[1][1]); SB();
      acc2[0][2] = mfma16(hr[4][0], wf[4][2], acc2[0][2]); SB();
      acc2[1][2] = mfma16(hr[4][1], wf[4][2], acc2[1][2]); SB();
      acc2[0][3] = mfma16(hr[4][0], wf[4][3], acc2[0][3]); SB();
      acc2[1][3] = mfma16(hr[4][1], wf[4][3], acc2[1][3]); SB();
      acc2[0][4] = mfma16(hr[4][0], wf[4][4], acc2[0][4]); SB();
      acc2[1][4] = mfma16(hr[4][1], wf[4][4], acc2[1][4]); SB();
      acc2[0][5] = mfma16(hr[4][0], wf[4][5], acc2[0][5]); SB();
      acc2[1][5] = mfma16(hr[4][1], wf[4][5], acc2[1][5]); SB();
      HPIN(5) nb = HBAD(5); SB();
      acc2[0][6] = mfma16(hr[4][0], wf[4][6], acc2[0][6]); SB();
      acc2[1][6] = mfma16(hr[4][1], wf[4][6], acc2[1][6]); SB();
      acc2[0][7] = mfma16(hr[4][0], wf[4][7], acc2[0][7]); SB();
      acc2[1][7] = mfma16(hr[4][1], wf[4][7], acc2[1][7]); SB();
      if (__builtin_expect(nb, 0)) {
        _Pragma("nounroll") for (unsigned spins = 0; spins < SPIN_CAP; ++spins) {
          CNT(cnt_h); __builtin_amdgcn_s_sleep(2); asm volatile("" ::: "memory");
          LDH(5)
          if (!HBAD(5)) break;
        }
        LDH(6) SB();
      }
      SB();
      acc2[0][0] = mfma16(hr[5][0], wf[5][0], acc2[0][0]); SB();
      r2a = w * 32768 + 24576 + lane * 16; r2b = 131072 + w * 8192 + lane * 16; asm volatile("" : "+v"(r2a), "+v"(r2b)); SB();
      acc2[1][0] = mfma16(hr[5][1], wf[5][0], acc2[1][0]); SB();
      acc2[0][1] = mfma16(hr[5][0], wf[5][1], acc2[0][1]); SB();
      r2r[0] = (w < 2 ? 24576 + w * 4096 : 131072 + (w - 2) * 4096) + lane * 16; r2r[1] = r2r[0] + (w < 2 ? 32768 : 8192);
      asm volatile("" : "+v"(r2r[0]), "+v"(r2r[1])); SB();
      acc2[1][1] = mfma16(hr[5][1], wf[5][1], acc2[1][1]); SB();
      acc2[0][2] = mfma16(hr[5][0], wf[5][2], acc2[0][2]); SB();
      r2r[2] = r2r[0] + (w < 2 ? 65536 : 16384); r2r[3] = r2r[0] + (w < 2 ? 98304 : 24576);
      asm volatile("" : "+v"(r2r[2]), "+v"(r2r[3])); SB();
      acc2[1][2] = mfma16(hr[5][1], wf[5][2], acc2[1][2]); SB();
      acc2[0][3] = mfma16(hr[5][0], wf[5][3], acc2[0][3]); SB();
      acc2[1][3] = mfma16(hr[5][1], wf[5][3], acc2[1][3]); SB();
      acc2[0][4] = mfma16(hr[5][0], wf[5][4], acc2[0][4]); SB();
      acc2[1][4] = mfma16(hr[5][1], wf[5][4], acc2[1][4]); SB();
      acc2[0][5] = mfma16(hr[5][0], wf[5][5], acc2[0][5]); SB();
      acc2[1][5] = mfma16(hr[5][1], wf[5][5], acc2[1][5]); SB();
      HPIN(6) nb = HBAD(6); SB();
      acc2[0][6] = mfma16(hr[5][0], wf[5][6], acc2[0][6]); SB();
      acc2[1][6] = mfma16(hr[5][1], wf[5][6], acc2[1][6]); SB();
      acc2[0][7] = mfma16(hr[5][0], wf[5][7], acc2[0][7]); SB();
      acc2[1][7] = mfma16(hr[5][1], wf[5][7], acc2[1][7]); SB();
      if (__builtin_expect(nb, 0)) {
        _Pragma("nounroll") for (unsigned spins = 0; spins < SPIN_CAP; ++spins) {
          CNT(cnt_h); __builtin_amdgcn_s_sleep(2); asm volatile("" ::: "memory");
          LDH(6)
          if (!HBAD(6)) break;
        }
      }
      const unsigned xrt = (unsigned)__builtin_amdgcn_s_memrealtime();
      SB();
      acc2[0][0] = mfma16(hr[6][0], wf[6][0], acc2[0][0]); SB();
      acc2[1][0] = mfma16(hr[6][1], wf[6][0], acc2[1][0]); SB();
      acc2[0][1] = mfma16(hr[6][0], wf[6][1], acc2[0][1]); SB();
      acc2[1][1] = mfma16(hr[6][1], wf[6][1], acc2[1][1]); SB();
      XANNT(); SB(); XSTAMP(7)
      acc2[0][2] = mfma16(hr[6][0], wf[6][2], acc2[0][2]); SB();
      *(f32x4*)(smem + r2a + 0 + 0) = acc2[0][0]; SB();
      acc2[1][2] = mfma16(hr[6][1], wf[6][2], acc2[1][2]); SB();
      *(f32x4*)(smem + r2b + 0 + 0) = acc2[1][0]; SB();
      acc2[0][3] = mfma16(hr[6][0], wf[6][3], acc2[0][3]); SB();
      *(f32x4*)(smem + r2a + 0 + 1024) = acc2[0][1]; SB();
      acc2[1][3] = mfma16(hr[6][1], wf[6][3], acc2[1][3]); SB();
      *(f32x4*)(smem + r2b + 0 + 1024) = acc2[1][1]; SB();
      acc2[0][4] = mfma16(hr[6][0], wf[6][4], acc2[0][4]); SB();
      *(f32x4*)(smem + r2a + 0 + 2048) = acc2[0][2]; SB();
      acc2[1][4] = mfma16(hr[6][1], wf[6][4], acc2[1][4]); SB();
      *(f32x4*)(smem + r2b + 0 + 2048) = acc2[1][2]; SB();
      acc2[0][5] = mfma16(hr[6][0], wf[6][5], acc2[0][5]); SB();
      *(f32x4*)(smem + r2a + 0 + 3072) = acc2[0][3]; SB();
      acc2[1][5] = mfma16(hr[6][1], wf[6][5], acc2[1][5]); SB();
      *(f32x4*)(smem + r2b + 0 + 3072) = acc2[1][3]; SB();
      acc2[0][6] = mfma16(hr[6][0], wf[6][6], acc2[0][6]); SB();
      *(f32x4*)(smem + r2a + 4096 + 0) = acc2[0][4]; SB();
      acc2[1][6] = mfma16(hr[6][1], wf[6][6], acc2[1][6]); SB();
      *(f32x4*)(smem + r2b + 4096 + 0) = acc2[1][4]; SB();
      acc2[0][7] = mfma16(hr[6][0], wf[6][7], acc2[0][7]); SB();
      *(f32x4*)(smem + r2a + 4096 + 1024) = acc2[0][5]; SB();
      acc2[1][7] = mfma16(hr[6][1], wf[6][7], acc2[1][7]); SB();
      *(f32x4*)(smem + r2b + 4096 + 1024) = acc2[1][5]; SB();
      STAMP(14)
      *(f32x4*)(smem + r2a + 4096 + 2048) = acc2[0][6]; SB(); *(f32x4*)(smem + r2b + 4096 + 2048) = acc2[1][6]; SB();
      *(f32x4*)(smem + r2a + 4096 + 3072) = acc2[0][7]; SB(); *(f32x4*)(smem + r2b + 4096 + 3072) = acc2[1][7]; SB();
      f32x4 pp[4][4], fin2[4];
      LDXG(); SB();
      BAR();
      XSTAMP(2)
      pp[0][0] = *(const f32x4*)(smem + r2r[0] + 0); pp[0][1] = *(const f32x4*)(smem + r2r[1] + 0); pp[0][2] = *(const f32x4*)(smem + r2r[2] + 0); pp[0][3] = *(const f32x4*)(smem + r2r[3] + 0); SB();
      pp[1][0] = *(const f32x4*)(smem + r2r[0] + 1024); pp[1][1] = *(const f32x4*)(smem + r2r[1] + 1024); pp[1][2] = *(const f32x4*)(smem + r2r[2] + 1024); pp[1][3] = *(const f32x4*)(smem + r2r[3] + 1024); SB();
      pp[2][0] = *(const f32x4*)(smem + r2r[0] + 2048); pp[2][1] = *(const f32x4*)(smem + r2r[1] + 2048); pp[2][2] = *(const f32x4*)(smem + r2r[2] + 2048); pp[2][3] = *(const f32x4*)(smem + r2r[3] + 2048); SB();
      pp[3][0] = *(const f32x4*)(smem + r2r[0] + 3072); pp[3][1] = *(const f32x4*)(smem + r2r[1] + 3072); pp[3][2] = *(const f32x4*)(smem + r2r[2] + 3072); pp[3][3] = *(const f32x4*)(smem + r2r[3] + 3072); SB();
      fin2[0] = ((pp[0][0] + pp[0][1]) + pp[0][2]) + pp[0][3];
      fin2[1] = ((pp[1][0] + pp[1][1]) + pp[1][2]) + pp[1][3];
      fin2[2] = ((pp[2][0] + pp[2][1]) + pp[2][2]) + pp[2][3];
      fin2[3] = ((pp[3][0] + pp[3][1]) + pp[3][2]) + pp[3][3];
      XSTAMP3(5, fin2[0], fin2[1], fin2[3])
      asm volatile("" : "+v"(fin2[0]), "+v"(fin2[1]), "+v"(fin2[2]), "+v"(fin2[3])); SB();
      v2u xg1 = xg; LDXG(); SB();
      v4u xv_[3];
      xv_[0] = pack24(fin2[0][1], fin2[1][1], fin2[2][1], fin2[3][1], t32); asm volatile("" : "+v"(xv_[0])); __builtin_amdgcn_raw_buffer_store_b128(xv_[0], rs, so_w, xo[0], SC1); SB();
      xv_[1] = pack24(fin2[0][2], fin2[1][2], fin2[2][2], fin2[3][2], t32); asm volatile("" : "+v"(xv_[1])); __builtin_amdgcn_raw_buffer_store_b128(xv_[1], rs, so_w, xo[1], SC1); SB();
      xv_[2] = pack24(fin2[0][3], fin2[1][3], fin2[2][3], fin2[3][3], t32); asm volatile("" : "+v"(xv_[2])); __builtin_amdgcn_raw_buffer_store_b128(xv_[2], rs, so_w, xo[2], SC1); SB();
#define LDS_(k) sl[k] = __builtin_amdgcn_raw_buffer_load_b128(rs, so_w, xin[k], SC1);
      SB();
      XSTAMP(3)
      if (!XTOK1()) {
        asm volatile("" ::: "memory"); xg1 = xg;
        if (!XTOK1())
        _Pragma("nounroll") for (unsigned spins = 0; spins < SPIN_CAP; ++spins) {
          __builtin_amdgcn_s_sleep(1); asm volatile("" ::: "memory"); xg1 = __builtin_amdgcn_raw_buffer_load_b64(rs, xfi, 0, SC1); CNT(cnt_poll);
          if (XTOK1()) break;
        }
      }
      _Pragma("nounroll") for (unsigned spins = 0; spins < 1024; ++spins) {
        if (__all((unsigned)__builtin_amdgcn_s_memrealtime() - xg1[1] - 64u < 0x80000000u)) break;
      }
      asm volatile("" ::: "memory");
      if (false) {
        _Pragma("nounroll") for (unsigned spins = 0; spins < SPIN_CAP; ++spins) {
          __builtin_amdgcn_s_sleep(1); asm volatile("" ::: "memory"); LDXG(); CNT(cnt_poll);
          if (XGOK()) break;
        }
        asm volatile("" ::: "memory");
      }
      XSTAMP(6)
      LDS_(0) LDS_(1) LDS_(2)
      SB();
      f32x4 o;
      _Pragma("unroll") for (int cc = 0; cc < 4; ++cc) o[cc] = fin2[cc][0];
      asm volatile("" : "+v"(o), "+v"(oo));
      SB();
      XSTAMP3(4, sl[0], sl[1], sl[2])
      if (__builtin_expect(__any((tag24(sl[0]) != t32) | (tag24(sl[1]) != t32) | (tag24(sl[2]) != t32)), 0)) {
        _Pragma("nounroll") for (unsigned spins = 0; spins < SPIN_CAP; ++spins) {
          CNT(cnt_x); __builtin_amdgcn_s_sleep(1); asm volatile("" ::: "memory");
          LDS_(0) LDS_(1) LDS_(2)
          if (!__any((tag24(sl[0]) != t32) | (tag24(sl[1]) != t32) | (tag24(sl[2]) != t32))) break;
        }
      }
#undef LDS_
      _Pragma("unroll") for (int k = 0; k < 3; ++k) o += unpack24(sl[k]);
      v2u v;
      v.x = cvt_pk(o[0], o[1]);
      v.y = cvt_pk(o[2], o[3]);
      *(GAS v2u*)((GAS unsigned char*)out + (unsigned)oo) = v;
    }
  }
  int bid2 = bid;
  asm volatile("" : "+s"(bid2));
  if (bid2 >= 224) {
    unsigned long long t0 = __builtin_amdgcn_s_memtime();
    const int u16 = (r - 28) * 4 + w;
    const __amdgpu_buffer_rsrc_t rx = __builtin_amdgcn_make_buffer_rsrc((void*)x, 0, M_ * D_ * 2, 0x00020000);
    constexpr int NTX = 1 == 1 ? 4 : 1 == 2 ? 2 : 1 == 3 ? 3 : 0;
    unsigned tx[4];
#define XWARM() _Pragma("unroll") for (int t = 0; t < NTX; ++t) tx[t] = __builtin_amdgcn_raw_buffer_load_b32(rx, \
      1 == 1 ? (u16 * 256 + t * 64 + lane) * 128 : (u16 * 16 + (lane >> 2)) * 2048 + (4 - NTX + t) * 512 + (lane & 3) * 128, 0, 0); SB();
    XWARM()
    unsigned tb[2];
    {
      const int jb = (3 - (int)((size_t)wg >> 7) - 7 * xcd) & 7;
      const GAS unsigned* pb = (const GAS unsigned*)((lane & 1) ? wu : wg) + (xcd * 7 + (jb < 7 ? jb : 0)) * 32;
#pragma unroll
      for (int t = 0; t < 2; ++t) {
        const int L = min((t * 16 + u16) * 64 + lane, 2 * (1024 - 256) - 1);
        tb[t] = pb[(size_t)(1 ? 256 + (L >> 1) : 256 * ((L >> 1) & 3) + 64 + (L >> 3)) * (F_ / 2)];
      }
    }
    constexpr int NLP = (1024 - 640) / 64;
    unsigned tl[NLP];
#define LATEW() { const GAS unsigned* pl = (const GAS unsigned*)((lane & 1) ? wu : wg) + (xcd * 7 + min((lane & 15) >> 1, 6)) * 32 \
        + (size_t)(640 + u16 * 4 + (lane >> 4)) * (F_ / 2); \
      _Pragma("unroll") for (int t = 0; t < NLP; ++t) tl[t] = pl[(size_t)t * 64 * (F_ / 2)]; SB(); }
    SB();
    COMMON()
    const int uh = (bid - 224) * 4 + w;
#define HARMH() { const int ha = (int)H_OFF + uh * 14336 + lane * 16; \
      _Pragma("unroll") for (int t = 0; t < 14; ++t) __builtin_amdgcn_raw_buffer_store_b128(SENT, rs, ha, t * 1024, SC1); SB(); }
#define HARMR(g0, g1) { _Pragma("unroll") for (int gq = g0; gq < g1; ++gq) { \
        const int dest = 2 * (uh & 1) + gq / 3, so_ = (((uh >> 1) * 4 + dest) * 4 + (dest ^ (gq % 3 + 1))) * 4096; \
        _Pragma("unroll") for (int c = 0; c < 4; ++c) __builtin_amdgcn_raw_buffer_store_b128(SENT, rs, (int)SLAB_OFF + lane * 16 + c * 1024, so_, SC1); } SB(); }
#define HARMS() HARMR(0, 6)
#define FLAG2() { const v2u tv = {(unsigned)tag, (unsigned)(tag >> 32)}; \
      __builtin_amdgcn_raw_buffer_store_b64(tv, rs, lane == 0 ? (int)AF2_OFF + uh * 8 : 0x7ffffff0, 0, SC1); }
    HARMH()
    STAMP(1)
#define TOUCHED() { _Pragma("unroll") for (int t = 0; t < NTX; ++t) asm volatile("" :: "v"(tx[t])); \
      if (1) asm volatile("" :: "v"(tb[0]), "v"(tb[1])); \
      if (2 == 1) { _Pragma("unroll") for (int t = 0; t < NLP; ++t) asm volatile("" :: "v"(tl[t])); } }
    VMWAIT(0);
    TOUCHED()
    STAMP(3)
    PINTAG();
    FLAG();
    {
      while (__builtin_amdgcn_s_memtime() - t0 < 2500) __builtin_amdgcn_s_sleep(2);
      LATEW()
      while (__builtin_amdgcn_s_memtime() - t0 < 7500) __builtin_amdgcn_s_sleep(4);
      unsigned td[7];
#pragma unroll
      for (int t = 0; t < 0; ++t) {
        const int id = ((u16 * 7 + t) * 64 + lane);
        td[t] = __builtin_amdgcn_raw_buffer_load_b32(rd, (896 * s2 + (id >> 3)) * (D_ * 2) + (xcd & 1) * 1024 + (id & 7) * 128, 0, 0);
      }
      STAMP(4)
      WD4();
#pragma unroll
      for (int n = 0; n < 30; ++n) LDW2(n);
#pragma unroll
      for (int n = 30; n < 56; ++n) LDW2(n);
      SB();
#pragma unroll
      for (int t = 0; t < 0; ++t) asm volatile("" :: "v"(td[t]));
#pragma unroll
      for (int t = 0; t < NLP; ++t) asm volatile("" :: "v"(tl[t]));
    }
    STAMP(6)
#define EFL(k) __builtin_amdgcn_raw_buffer_load_b128(rs, (int)AF_OFF + lane * 16, (k) * 1024, SC1)
#define EFM(f) ((f[0] == (unsigned)tag) & (f[1] == (unsigned)(tag >> 32)) & (f[2] == (unsigned)tag) & (f[3] == (unsigned)(tag >> 32)))
#define EST() if (lane == 0) __hip_atomic_store((GAS u64*)(ws + EP_OFF), tag, RLX_AGENT);
#define EPOCH() { _Pragma("nounroll") for (unsigned spins = 0; spins < SPIN_CAP; ++spins) { bool m = true; \
      _Pragma("unroll") for (int k = 0; k < 8; ++k) { const v4u f = EFL(k); m &= EFM(f); } \
      if (__all(m)) { EST() break; } \
      __builtin_amdgcn_s_sleep(4); asm volatile("" ::: "memory"); } }
#define EPADV() if (adv) { bool m = true; _Pragma("unroll") for (int k = 0; k < 8; ++k) m &= EFM(ef[k]); \
      if (__all(m)) { EST() adv = false; } }
    bool adv = bid == 255 && w == 0;
    v4u ef[8];
    if (adv) { _Pragma("unroll") for (int k = 0; k < 8; ++k) ef[k] = EFL(k); }
    SB();
    LDG();
    {
      v4u sl[3], wf[7][8];
      int hbase1 = hbase + KS2 * 1024;
      asm volatile("" : "+v"(hbase1));
      int xfo = lane == 0 ? (int)XF_OFF + bid * 128 + w * 8 : 0x7ffffff0;
      int xfi = (int)XF_OFF + (r * 8 + 2 * (s2 ^ (((lane >> 2) & 3) % 3 + 1)) + (xcd & 1)) * 128 + (lane & 3) * 8;
      asm volatile("" : "+v"(xfo), "+v"(xfi));
      v2u xg;
      unsigned t32 = ((unsigned)tag + (unsigned)(tag >> 32) * 0x85EBCA6Bu) * 0x9E3779B1u; asm volatile("" : "+s"(t32));
      SB();
      WFQ(0)
      WFQ(1)
      WFQ(2)
      WFQ(3)
      WFQ(4)
      WFQ(5)
      WFQ(6)
      SB();
      if (!GOK()) GWAIT()
      while (__builtin_amdgcn_s_memtime() - t0 < 16000) __builtin_amdgcn_s_sleep(4); asm volatile("" ::: "memory");
      LDH(0) SB();
      LDH(1) SB();
      LDH(2) SB();
      SB();
      SB();
      LDH(3) SB();
      LDH(4) SB();
      LDH(5) SB();
      LDH(6) SB();
      SB();
      EPADV() SB();
      STAMP(12)
      unsigned r2a, r2b, r2r[4];
      f32x4 acc2[2][8];
      HPIN(0) bool nb = HBAD(0);
      if (__builtin_expect(nb, 0)) {
        _Pragma("nounroll") for (unsigned spins = 0; spins < SPIN_CAP; ++spins) {
          CNT(cnt_h); __builtin_amdgcn_s_sleep(2); asm volatile("" ::: "memory");
          LDH(0)
          if (!HBAD(0)) break;
        }
        LDH(1) SB();
        LDH(2) SB();
        LDH(3) SB();
        LDH(4) SB();
        LDH(5) SB();
        LDH(6) SB();
      }
      STAMP(13)
      SB();
      acc2[0][0] = mfma16(hr[0][0], wf[0][0], f32x4{0.f, 0.f, 0.f, 0.f}); SB();
      acc2[1][0] = mfma16(hr[0][1], wf[0][0], f32x4{0.f, 0.f, 0.f, 0.f}); SB();
      acc2[0][1] = mfma16(hr[0][0], wf[0][1], f32x4{0.f, 0.f, 0.f, 0.f}); SB();
      acc2[1][1] = mfma16(hr[0][1], wf[0][1], f32x4{0.f, 0.f, 0.f, 0.f}); SB();
      acc2[0][2] = mfma16(hr[0][0], wf[0][2], f32x4{0.f, 0.f, 0.f, 0.f}); SB();
      acc2[1][2] = mfma16(hr[0][1], wf[0][2], f32x4{0.f, 0.f, 0.f, 0.f}); SB();
      acc2[0][3] = mfma16(hr[0][0], wf[0][3], f32x4{0.f, 0.f, 0.f, 0.f}); SB();
      acc2[1][3] = mfma16(hr[0][1], wf[0][3], f32x4{0.f, 0.f, 0.f, 0.f}); SB();
      acc2[0][4] = mfma16(hr[0][0], wf[0][4], f32x4{0.f, 0.f, 0.f, 0.f}); SB();
      acc2[1][4] = mfma16(hr[0][1], wf[0][4], f32x4{0.f, 0.f, 0.f, 0.f}); SB();
      acc2[0][5] = mfma16(hr[0][0], wf[0][5], f32x4{0.f, 0.f, 0.f, 0.f}); SB();
      acc2[1][5] = mfma16(hr[0][1], wf[0][5], f32x4{0.f, 0.f, 0.f, 0.f}); SB();
      HPIN(1) nb = HBAD(1); SB();
      acc2[0][6] = mfma16(hr[0][0], wf[0][6], f32x4{0.f, 0.f, 0.f, 0.f}); SB();
      acc2[1][6] = mfma16(hr[0][1], wf[0][6], f32x4{0.f, 0.f, 0.f, 0.f}); SB();
      acc2[0][7] = mfma16(hr[0][0], wf[0][7], f32x4{0.f, 0.f, 0.f, 0.f}); SB();
      acc2[1][7] = mfma16(hr[0][1], wf[0][7], f32x4{0.f, 0.f, 0.f, 0.f}); SB();
      if (__builtin_expect(nb, 0)) {
        _Pragma("nounroll") for (unsigned spins = 0; spins < SPIN_CAP; ++spins) {
          CNT(cnt_h); __builtin_amdgcn_s_sleep(2); asm volatile("" ::: "memory");
          LDH(1)
          if (!HBAD(1)) break;
        }
        LDH(2) SB();
        LDH(3) SB();
        LDH(4) SB();
        LDH(5) SB();
        LDH(6) SB();
      }
      SB();
      acc2[0][0] = mfma16(hr[1][0], wf[1][0], acc2[0][0]); SB();
      acc2[1][0] = mfma16(hr[1][1], wf[1][0], acc2[1][0]); SB();
      acc2[0][1] = mfma16(hr[1][0], wf[1][1], acc2[0][1]); SB();
      acc2[1][1] = mfma16(hr[1][1], wf[1][1], acc2[1][1]); SB();
      acc2[0][2] = mfma16(hr[1][0], wf[1][2], acc2[0][2]); SB();
      acc2[1][2] = mfma16(hr[1][1], wf[1][2], acc2[1][2]); SB();
      acc2[0][3] = mfma16(hr[1][0], wf[1][3], acc2[0][3]); SB();
      acc2[1][3] = mfma16(hr[1][1], wf[1][3], acc2[1][3]); SB();
      acc2[0][4] = mfma16(hr[1][0], wf[1][4], acc2[0][4]); SB();
      acc2[1][4] = mfma16(hr[1][1], wf[1][4], acc2[1][4]); SB();
      acc2[0][5] = mfma16(hr[1][0], wf[1][5], acc2[0][5]); SB();
      acc2[1][5] = mfma16(hr[1][1], wf[1][5], acc2[1][5]); SB();
      HPIN(2) nb = HBAD(2); SB();
      acc2[0][6] = mfma16(hr[1][0], wf[1][6], acc2[0][6]); SB();
      acc2[1][6] = mfma16(hr[1][1], wf[1][6], acc2[1][6]); SB();
      acc2[0][7] = mfma16(hr[1][0], wf[1][7], acc2[0][7]); SB();
      acc2[1][7] = mfma16(hr[1][1], wf[1][7], acc2[1][7]); SB();
      if (__builtin_expect(nb, 0)) {
        _Pragma("nounroll") for (unsigned spins = 0; spins < SPIN_CAP; ++spins) {
          CNT(cnt_h); __builtin_amdgcn_s_sleep(2); asm volatile("" ::: "memory");
          LDH(2)
          if (!HBAD(2)) break;
        }
        LDH(3) SB();
        LDH(4) SB();
        LDH(5) SB();
        LDH(6) SB();
      }
      SB();
      acc2[0][0] = mfma16(hr[2][0], wf[2][0], acc2[0][0]); SB();
      acc2[1][0] = mfma16(hr[2][1], wf[2][0], acc2[1][0]); SB();
      acc2[0][1] = mfma16(hr[2][0], wf[2][1], acc2[0][1]); SB();
      acc2[1][1] = mfma16(hr[2][1], wf[2][1], acc2[1][1]); SB();
      acc2[0][2] = mfma16(hr[2][0], wf[2][2], acc2[0][2]); SB();
      acc2[1][2] = mfma16(hr[2][1], wf[2][2], acc2[1][2]); SB();
      acc2[0][3] = mfma16(hr[2][0], wf[2][3], acc2[0][3]); SB();
      acc2[1][3] = mfma16(hr[2][1], wf[2][3], acc2[1][3]); SB();
      acc2[0][4] = mfma16(hr[2][0], wf[2][4], acc2[0][4]); SB();
      acc2[1][4] = mfma16(hr[2][1], wf[2][4], acc2[1][4]); SB();
      acc2[0][5] = mfma16(hr[2][0], wf[2][5], acc2[0][5]); SB();
      acc2[1][5] = mfma16(hr[2][1], wf[2][5], acc2[1][5]); SB();
      HPIN(3) nb = HBAD(3); SB();
      acc2[0][6] = mfma16(hr[2][0], wf[2][6], acc2[0][6]); SB();
      acc2[1][6] = mfma16(hr[2][1], wf[2][6], acc2[1][6]); SB();
      acc2[0][7] = mfma16(hr[2][0], wf[2][7], acc2[0][7]); SB();
      acc2[1][7] = mfma16(hr[2][1], wf[2][7], acc2[1][7]); SB();
      if (__builtin_expect(nb, 0)) {
        _Pragma("nounroll") for (unsigned spins = 0; spins < SPIN_CAP; ++spins) {
          CNT(cnt_h); __builtin_amdgcn_s_sleep(2); asm volatile("" ::: "memory");
          LDH(3)
          if (!HBAD(3)) break;
        }
        LDH(4) SB();
        LDH(5) SB();
        LDH(6) SB();
      }
      SB();
      acc2[0][0] = mfma16(hr[3][0], wf[3][0], acc2[0][0]); SB();
      acc2[1][0] = mfma16(hr[3][1], wf[3][0], acc2[1][0]); SB();
      acc2[0][1] = mfma16(hr[3][0], wf[3][1], acc2[0][1]); SB();
      acc2[1][1] = mfma16(hr[3][1], wf[3][1], acc2[1][1]); SB();
      acc2[0][2] = mfma16(hr[3][0], wf[3][2], acc2[0][2]); SB();
      acc2[1][2] = mfma16(hr[3][1], wf[3][2], acc2[1][2]); SB();
      acc2[0][3] = mfma16(hr[3][0], wf[3][3], acc2[0][3]); SB();
      acc2[1][3] = mfma16(hr[3][1], wf[3][3], acc2[1][3]); SB();
      acc2[0][4] = mfma16(hr[3][0], wf[3][4], acc2[0][4]); SB();
      acc2[1][4] = mfma16(hr[3][1], wf[3][4], acc2[1][4]); SB();
      acc2[0][5] = mfma16(hr[3][0], wf[3][5], acc2[0][5]); SB();
      acc2[1][5] = mfma16(hr[3][1], wf[3][5], acc2[1][5]); SB();
      HPIN(4) nb = HBAD(4); SB();
      acc2[0][6] = mfma16(hr[3][0], wf[3][6], acc2[0][6]); SB();
      acc2[1][6] = mfma16(hr[3][1], wf[3][6], acc2[1][6]); SB();
      acc2[0][7] = mfma16(hr[3][0], wf[3][7], acc2[0][7]); SB();
      acc2[1][7] = mfma16(hr[3][1], wf[3][7], acc2[1][7]); SB();
      if (__builtin_expect(nb, 0)) {
        _Pragma("nounroll") for (unsigned spins = 0; spins < SPIN_CAP; ++spins) {
          CNT(cnt_h); __builtin_amdgcn_s_sleep(2); asm volatile("" ::: "memory");
          LDH(4)
          if (!HBAD(4)) break;
        }
        LDH(5) SB();
        LDH(6) SB();
      }
      SB();
      acc2[0][0] = mfma16(hr[4][0], wf[4][0], acc2[0][0]); SB();
      acc2[1][0] = mfma16(hr[4][1], wf[4][0], acc2[1][0]); SB();
      acc2[0][1] = mfma16(hr[4][0], wf[4][1], acc2[0][1]); SB();
      acc2[1][1] = mfma16(hr[4][1], wf[4][1], acc2[1][1]); SB();
      acc2[0][2] = mfma16(hr[4][0], wf[4][2], acc2[0][2]); SB();
      acc2[1][2] = mfma16(hr[4][1], wf[4][2], acc2[1][2]); SB();
      acc2[0][3] = mfma16(hr[4][0], wf[4][3], acc2[0][3]); SB();
      acc2[1][3] = mfma16(hr[4][1], wf[4][3], acc2[1][3]); SB();
      acc2[0][4] = mfma16(hr[4][0], wf[4][4], acc2[0][4]); SB();
      acc2[1][4] = mfma16(hr[4][1], wf[4][4], acc2[1][4]); SB();
      acc2[0][5] = mfma16(hr[4][0], wf[4][5], acc2[0][5]); SB();
      acc2[1][5] = mfma16(hr[4][1], wf[4][5], acc2[1][5]); SB();
      HPIN(5) nb = HBAD(5); SB();
      acc2[0][6] = mfma16(hr[4][0], wf[4][6], acc2[0][6]); SB();
      acc2[1][6] = mfma16(hr[4][1], wf[4][6], acc2[1][6]); SB();
      acc2[0][7] = mfma16(hr[4][0], wf[4][7], acc2[0][7]); SB();
      acc2[1][7] = mfma16(hr[4][1], wf[4][7], acc2[1][7]); SB();
      if (__builtin_expect(nb, 0)) {
        _Pragma("nounroll") for (unsigned spins = 0; spins < SPIN_CAP; ++spins) {
          CNT(cnt_h); __builtin_amdgcn_s_sleep(2); asm volatile("" ::: "memory");
          LDH(5)
          if (!HBAD(5)) break;
        }
        LDH(6) SB();
      }
      SB();
      acc2[0][0] = mfma16(hr[5][0], wf[5][0], acc2[0][0]); SB();
      r2a = w * 32768 + 24576 + lane * 16; r2b = 131072 + w * 8192 + lane * 16; asm volatile("" : "+v"(r2a), "+v"(r2b)); SB();
      acc2[1][0] = mfma16(hr[5][1], wf[5][0], acc2[1][0]); SB();
      acc2[0][1] = mfma16(hr[5][0], wf[5][1], acc2[0][1]); SB();
      r2r[0] = (w < 2 ? 24576 + w * 4096 : 131072 + (w - 2) * 4096) + lane * 16; r2r[1] = r2r[0] + (w < 2 ? 32768 : 8192);
      asm volatile("" : "+v"(r2r[0]), "+v"(r2r[1])); SB();
      acc2[1][1] = mfma16(hr[5][1], wf[5][1], acc2[1][1]); SB();
      acc2[0][2] = mfma16(hr[5][0], wf[5][2], acc2[0][2]); SB();
      r2r[2] = r2r[0] + (w < 2 ? 65536 : 16384); r2r[3] = r2r[0] + (w < 2 ? 98304 : 24576);
      asm volatile("" : "+v"(r2r[2]), "+v"(r2r[3])); SB();
      acc2[1][2] = mfma16(hr[5][1], wf[5][2], acc2[1][2]); SB();
      acc2[0][3] = mfma16(hr[5][0], wf[5][3], acc2[0][3]); SB();
      acc2[1][3] = mfma16(hr[5][1], wf[5][3], acc2[1][3]); SB();
      acc2[0][4] = mfma16(hr[5][0], wf[5][4], acc2[0][4]); SB();
      acc2[1][4] = mfma16(hr[5][1], wf[5][4], acc2[1][4]); SB();
      acc2[0][5] = mfma16(hr[5][0], wf[5][5], acc2[0][5]); SB();
      acc2[1][5] = mfma16(hr[5][1], wf[5][5], acc2[1][5]); SB();
      HPIN(6) nb = HBAD(6); SB();
      acc2[0][6] = mfma16(hr[5][0], wf[5][6], acc2[0][6]); SB();
      acc2[1][6] = mfma16(hr[5][1], wf[5][6], acc2[1][6]); SB();
      acc2[0][7] = mfma16(hr[5][0], wf[5][7], acc2[0][7]); SB();
      acc2[1][7] = mfma16(hr[5][1], wf[5][7], acc2[1][7]); SB();
      if (__builtin_expect(nb, 0)) {
        _Pragma("nounroll") for (unsigned spins = 0; spins < SPIN_CAP; ++spins) {
          CNT(cnt_h); __builtin_amdgcn_s_sleep(2); asm volatile("" ::: "memory");
          LDH(6)
          if (!HBAD(6)) break;
        }
      }
      const unsigned xrt = (unsigned)__builtin_amdgcn_s_memrealtime();
      SB();
      acc2[0][0] = mfma16(hr[6][0], wf[6][0], acc2[0][0]); SB();
      acc2[1][0] = mfma16(hr[6][1], wf[6][0], acc2[1][0]); SB();
      acc2[0][1] = mfma16(hr[6][0], wf[6][1], acc2[0][1]); SB();
      acc2[1][1] = mfma16(hr[6][1], wf[6][1], acc2[1][1]); SB();
      XANNT(); SB(); XSTAMP(7)
      acc2[0][2] = mfma16(hr[6][0], wf[6][2], acc2[0][2]); SB();
      *(f32x4*)(smem + r2a + 0 + 0) = acc2[0][0]; SB();
      acc2[1][2] = mfma16(hr[6][1], wf[6][2], acc2[1][2]); SB();
      *(f32x4*)(smem + r2b + 0 + 0) = acc2[1][0]; SB();
      acc2[0][3] = mfma16(hr[6][0], wf[6][3], acc2[0][3]); SB();
      *(f32x4*)(smem + r2a + 0 + 1024) = acc2[0][1]; SB();
      acc2[1][3] = mfma16(hr[6][1], wf[6][3], acc2[1][3]); SB();
      *(f32x4*)(smem + r2b + 0 + 1024) = acc2[1][1]; SB();
      acc2[0][4] = mfma16(hr[6][0], wf[6][4], acc2[0][4]); SB();
      *(f32x4*)(smem + r2a + 0 + 2048) = acc2[0][2]; SB();
      acc2[1][4] = mfma16(hr[6][1], wf[6][4], acc2[1][4]); SB();
      *(f32x4*)(smem + r2b + 0 + 2048) = acc2[1][2]; SB();
      acc2[0][5] = mfma16(hr[6][0], wf[6][5], acc2[0][5]); SB();
      *(f32x4*)(smem + r2a + 0 + 3072) = acc2[0][3]; SB();
      acc2[1][5] = mfma16(hr[6][1], wf[6][5], acc2[1][5]); SB();
      *(f32x4*)(smem + r2b + 0 + 3072) = acc2[1][3]; SB();
      acc2[0][6] = mfma16(hr[6][0], wf[6][6], acc2[0][6]); SB();
      *(f32x4*)(smem + r2a + 4096 + 0) = acc2[0][4]; SB();
      acc2[1][6] = mfma16(hr[6][1], wf[6][6], acc2[1][6]); SB();
      *(f32x4*)(smem + r2b + 4096 + 0) = acc2[1][4]; SB();
      acc2[0][7] = mfma16(hr[6][0], wf[6][7], acc2[0][7]); SB();
      *(f32x4*)(smem + r2a + 4096 + 1024) = acc2[0][5]; SB();
      acc2[1][7] = mfma16(hr[6][1], wf[6][7], acc2[1][7]); SB();
      *(f32x4*)(smem + r2b + 4096 + 1024) = acc2[1][5]; SB();
      STAMP(14)
      *(f32x4*)(smem + r2a + 4096 + 2048) = acc2[0][6]; SB(); *(f32x4*)(smem + r2b + 4096 + 2048) = acc2[1][6]; SB();
      *(f32x4*)(smem + r2a + 4096 + 3072) = acc2[0][7]; SB(); *(f32x4*)(smem + r2b + 4096 + 3072) = acc2[1][7]; SB();
      f32x4 pp[4][4], fin2[4];
      LDXG(); SB();
      BAR();
      XSTAMP(2)
      pp[0][0] = *(const f32x4*)(smem + r2r[0] + 0); pp[0][1] = *(const f32x4*)(smem + r2r[1] + 0); pp[0][2] = *(const f32x4*)(smem + r2r[2] + 0); pp[0][3] = *(const f32x4*)(smem + r2r[3] + 0); SB();
      pp[1][0] = *(const f32x4*)(smem + r2r[0] + 1024); pp[1][1] = *(const f32x4*)(smem + r2r[1] + 1024); pp[1][2] = *(const f32x4*)(smem + r2r[2] + 1024); pp[1][3] = *(const f32x4*)(smem + r2r[3] + 1024); SB();
      pp[2][0] = *(const f32x4*)(smem + r2r[0] + 2048); pp[2][1] = *(const f32x4*)(smem + r2r[1] + 2048); pp[2][2] = *(const f32x4*)(smem + r2r[2] + 2048); pp[2][3] = *(const f32x4*)(smem + r2r[3] + 2048); SB();
      pp[3][0] = *(const f32x4*)(smem + r2r[0] + 3072); pp[3][1] = *(const f32x4*)(smem + r2r[1] + 3072); pp[3][2] = *(const f32x4*)(smem + r2r[2] + 3072); pp[3][3] = *(const f32x4*)(smem + r2r[3] + 3072); SB();
      fin2[0] = ((pp[0][0] + pp[0][1]) + pp[0][2]) + pp[0][3];
      fin2[1] = ((pp[1][0] + pp[1][1]) + pp[1][2]) + pp[1][3];
      fin2[2] = ((pp[2][0] + pp[2][1]) + pp[2][2]) + pp[2][3];
      fin2[3] = ((pp[3][0] + pp[3][1]) + pp[3][2]) + pp[3][3];
      XSTAMP3(5, fin2[0], fin2[1], fin2[3])
      asm volatile("" : "+v"(fin2[0]), "+v"(fin2[1]), "+v"(fin2[2]), "+v"(fin2[3])); SB();
      v2u xg1 = xg; LDXG(); SB();
      v4u xv_[3];
      xv_[0] = pack24(fin2[0][1], fin2[1][1], fin2[2][1], fin2[3][1], t32); asm volatile("" : "+v"(xv_[0])); __builtin_amdgcn_raw_buffer_store_b128(xv_[0], rs, so_w, xo[0], SC1); SB();
      xv_[1] = pack24(fin2[0][2], fin2[1][2], fin2[2][2], fin2[3][2], t32); asm volatile("" : "+v"(xv_[1])); __builtin_amdgcn_raw_buffer_store_b128(xv_[1], rs, so_w, xo[1], SC1); SB();
      xv_[2] = pack24(fin2[0][3], fin2[1][3], fin2[2][3], fin2[3][3], t32); asm volatile("" : "+v"(xv_[2])); __builtin_amdgcn_raw_buffer_store_b128(xv_[2], rs, so_w, xo[2], SC1); SB();
#define LDS_(k) sl[k] = __builtin_amdgcn_raw_buffer_load_b128(rs, so_w, xin[k], SC1);
      SB();
      XSTAMP(3)
      if (!XTOK1()) {
        asm volatile("" ::: "memory"); xg1 = xg;
        if (!XTOK1())
        _Pragma("nounroll") for (unsigned spins = 0; spins < SPIN_CAP; ++spins) {
          __builtin_amdgcn_s_sleep(1); asm volatile("" ::: "memory"); xg1 = __builtin_amdgcn_raw_buffer_load_b64(rs, xfi, 0, SC1); CNT(cnt_poll);
          if (XTOK1()) break;
        }
      }
      _Pragma("nounroll") for (unsigned spins = 0; spins < 1024; ++spins) {
        if (__all((unsigned)__builtin_amdgcn_s_memrealtime() - xg1[1] - 64u < 0x80000000u)) break;
      }
      asm volatile("" ::: "memory");
      if (false) {
        _Pragma("nounroll") for (unsigned spins = 0; spins < SPIN_CAP; ++spins) {
          __builtin_amdgcn_s_sleep(1); asm volatile("" ::: "memory"); LDXG(); CNT(cnt_poll);
          if (XGOK()) break;
        }
        asm volatile("" ::: "memory");
      }
      XSTAMP(6)
      LDS_(0) LDS_(1) LDS_(2)
      SB();
      f32x4 o;
      _Pragma("unroll") for (int cc = 0; cc < 4; ++cc) o[cc] = fin2[cc][0];
      asm volatile("" : "+v"(o), "+v"(oo));
      SB();
      XSTAMP3(4, sl[0], sl[1], sl[2])
      if (__builtin_expect(__any((tag24(sl[0]) != t32) | (tag24(sl[1]) != t32) | (tag24(sl[2]) != t32)), 0)) {
        _Pragma("nounroll") for (unsigned spins = 0; spins < SPIN_CAP; ++spins) {
          CNT(cnt_x); __builtin_amdgcn_s_sleep(1); asm volatile("" ::: "memory");
          LDS_(0) LDS_(1) LDS_(2)
          if (!__any((tag24(sl[0]) != t32) | (tag24(sl[1]) != t32) | (tag24(sl[2]) != t32))) break;
        }
      }
#undef LDS_
      _Pragma("unroll") for (int k = 0; k < 3; ++k) o += unpack24(sl[k]);
      v2u v;
      v.x = cvt_pk(o[0], o[1]);
      v.y = cvt_pk(o[2], o[3]);
      *(GAS v2u*)((GAS unsigned char*)out + (unsigned)oo) = v;
      if (adv) { EPOCH() }
    }
  }
  if (0 <= 1) { STAMP(15) }
}

extern "C" void kernel_launch(void* const* d_in, const int* in_sizes, int n_in, void* d_out, int out_size,
                              void* d_ws, size_t ws_size, hipStream_t stream) {
  k_fused<<<256, 256, 0, stream>>>((const unsigned short*)d_in[0], (const unsigned short*)d_in[1], (const unsigned short*)d_in[2],
                                   (const unsigned short*)d_in[3], (unsigned char*)d_ws, (unsigned short*)d_out);
}
```
